```python
import torch

def _softmax(x, axis=-1):
    return torch.softmax(x, dim=axis)
def _gelu(x):
    return torch.nn.functional.gelu(x, approximate="tanh")
def _silu(x):
    return torch.nn.functional.silu(x)
def _sigmoid(x):
    return torch.sigmoid(x)
def _layernorm(x, axis=-1, eps=1e-5):
    m = x.mean(dim=axis, keepdim=True)
    v = x.var(dim=axis, keepdim=True, unbiased=False)
    return (x - m) / torch.sqrt(v + eps)
def _scatter_add(tgt, idx, src, axis=0):
    return tgt.clone().index_add_(axis, idx, src)
def _segment_sum(src, seg, n):
    out = torch.zeros((n,) + src.shape[1:], dtype=src.dtype)
    return out.index_add_(0, seg, src)
def _pad(x, pw):
    flat = tuple(v for pair in reversed(pw) for v in pair)
    return torch.nn.functional.pad(x, flat)

kernel_name = 'standalone__qk_norm__prompt'

def setup_inputs():

    _w = (lambda a: (lambda t: t.to(torch.bfloat16) if t.is_floating_point() else t)(torch.as_tensor(a)))
    B, H, S, D = 2, 16, 2048, 448
    Dh = D // H
    return {
        "q": _w((lambda *s: torch.randn(*s, dtype=torch.bfloat16) * 0.05)(B, H, S, Dh)),
        "k": _w((lambda *s: torch.randn(*s, dtype=torch.bfloat16) * 0.05)(B, H, S, Dh)),
        "v": _w((lambda *s: torch.randn(*s, dtype=torch.bfloat16) * 1.6)(B, H, S, Dh)),
        "t": _w(torch.abs((lambda *s: torch.randn(*s, dtype=torch.bfloat16) * 1.6)(H)) + 1.0),
    }

def reference(q, k, v, t):
    eps = 1e-6
    qn = q / (torch.sqrt((q * q).sum(axis=-1, keepdims=True)) + eps)
    kn = k / (torch.sqrt((k * k).sum(axis=-1, keepdims=True)) + eps)
    logits = torch.einsum('bhqd,bhkd->bhqk', qn, kn) * t[None, :, None, None]
    attn = _softmax(logits, axis=-1)
    return torch.einsum('bhqk,bhkd->bhqd', attn, v)
```

```cpp
#include <hip/hip_runtime.h>
#include <hip/hip_bf16.h>

typedef float f32x16 __attribute__((ext_vector_type(16)));
typedef float f32x2 __attribute__((ext_vector_type(2)));
typedef __bf16 bf16x8 __attribute__((ext_vector_type(8)));
typedef __bf16 bf16x2 __attribute__((ext_vector_type(2)));
typedef unsigned u32x4 __attribute__((ext_vector_type(4)));
typedef unsigned u32x2 __attribute__((ext_vector_type(2)));

constexpr int NB = 2, NH = 16, SEQ = 2048, DH = 28;

__device__ __forceinline__ unsigned pk_bf16(float lo, float hi) {
    f32x2 v = {lo, hi};
    return __builtin_bit_cast(unsigned, __builtin_convertvector(v, bf16x2));
}
__device__ __forceinline__ float bf_lo(unsigned w) { return __uint_as_float(w << 16); }
__device__ __forceinline__ float bf_hi(unsigned w) { return __uint_as_float(w & 0xffff0000u); }
__device__ __forceinline__ float rbf(float x) { return bf_lo(pk_bf16(x, 0.f)); }
__device__ __forceinline__ float xhalf_add(float x) { return x + __shfl_xor(x, 32, 64); }
__device__ __forceinline__ float xhalf_max(float x) { return fmaxf(x, __shfl_xor(x, 32, 64)); }

__device__ __forceinline__ void load_norm_half_row(const unsigned* rowp, int h, unsigned (&o)[8]) {
    unsigned w[7];
#pragma unroll
    for (int i = 0; i < 7; ++i) w[i] = rowp[7 * h + i];
    float ss = 0.f;
#pragma unroll
    for (int i = 0; i < 7; ++i) { float a = bf_lo(w[i]), b = bf_hi(w[i]); ss += rbf(a * a); ss += rbf(b * b); }
    ss = rbf(xhalf_add(ss));
    const float den = rbf(rbf(sqrtf(ss)) + 1e-6f);
#pragma unroll
    for (int i = 0; i < 7; ++i) o[i] = pk_bf16(bf_lo(w[i]) / den, bf_hi(w[i]) / den);
    o[7] = 0u;
}

__device__ __forceinline__ void slow_path(const unsigned* q, const unsigned* k, const unsigned short* v, float t,
                                          unsigned* out, int qc, char* lds) {
    const int tid = threadIdx.x, lane = tid & 63, wid = tid >> 6, r = lane & 31, h = lane >> 5;
    u32x4* kl = (u32x4*)lds;
    unsigned short* vl = (unsigned short*)(lds + 16384);
    unsigned qf[8];
    load_norm_half_row(q + (size_t)(qc * 256 + wid * 32 + r) * 14, h, qf);
    const bf16x8 q0 = __builtin_bit_cast(bf16x8, (u32x4){qf[0], qf[1], qf[2], qf[3]});
    const bf16x8 q1 = __builtin_bit_cast(bf16x8, (u32x4){qf[4], qf[5], qf[6], qf[7]});
    f32x16 o = {};
    float m = -INFINITY, l = 0.f;
    for (int c = 0; c < SEQ / 256; ++c) {
        __syncthreads();
        unsigned kf[8];
        load_norm_half_row(k + (size_t)(c * 256 + wid * 32 + r) * 14, h, kf);
        kl[(wid * 2 + 0) * 64 + lane] = (u32x4){kf[0], kf[1], kf[2], kf[3]};
        kl[(wid * 2 + 1) * 64 + lane] = (u32x4){kf[4], kf[5], kf[6], kf[7]};
        for (int i = tid; i < 256 * 14; i += 512) ((unsigned*)vl)[i] = ((const unsigned*)v)[(size_t)c * 256 * 14 + i];
        __syncthreads();
        for (int T = 0; T < 8; ++T) {
            const bf16x8 k0 = __builtin_bit_cast(bf16x8, kl[(T * 2 + 0) * 64 + lane]);
            const bf16x8 k1 = __builtin_bit_cast(bf16x8, kl[(T * 2 + 1) * 64 + lane]);
            f32x16 s = {};
            s = __builtin_amdgcn_mfma_f32_32x32x16_bf16(k0, q0, s, 0, 0, 0);
            s = __builtin_amdgcn_mfma_f32_32x32x16_bf16(k1, q1, s, 0, 0, 0);
            float tm = -INFINITY;
#pragma unroll
            for (int i = 0; i < 16; ++i) { s[i] = rbf(rbf(s[i]) * t); tm = fmaxf(tm, s[i]); }
            tm = xhalf_max(tm);
            const float mn = fmaxf(m, tm);
            const float alpha = __expf(m - mn);
            m = mn;
            float ps = 0.f;
#pragma unroll
            for (int i = 0; i < 16; ++i) { s[i] = __expf(s[i] - mn); ps += s[i]; o[i] *= alpha; }
            l = l * alpha + ps;
#pragma unroll
            for (int st = 0; st < 2; ++st) {
                const bf16x8 p = __builtin_bit_cast(bf16x8, (u32x4){pk_bf16(s[8 * st], s[8 * st + 1]), pk_bf16(s[8 * st + 2], s[8 * st + 3]),
                                                                    pk_bf16(s[8 * st + 4], s[8 * st + 5]), pk_bf16(s[8 * st + 6], s[8 * st + 7])});
                unsigned vw[4];
#pragma unroll
                for (int j = 0; j < 4; ++j) {
                    const int key = T * 32 + 16 * st + 8 * (j >> 1) + 4 * h + 2 * (j & 1);
                    const int d = r < DH ? r : 0;
                    vw[j] = (unsigned)vl[key * DH + d] | ((unsigned)vl[(key + 1) * DH + d] << 16);
                }
                o = __builtin_amdgcn_mfma_f32_32x32x16_bf16(__builtin_bit_cast(bf16x8, (u32x4){vw[0], vw[1], vw[2], vw[3]}), p, o, 0, 0, 0);
            }
        }
    }
    l = xhalf_add(l);
    const float rl = 1.f / l;
    unsigned* orow = out + (size_t)(qc * 256 + wid * 32 + r) * 14;
#pragma unroll
    for (int g = 0; g < 4; ++g) {
        if (g < 3 || h == 0) {
            orow[4 * g + 2 * h] = pk_bf16(o[4 * g] * rl, o[4 * g + 1] * rl);
            orow[4 * g + 2 * h + 1] = pk_bf16(o[4 * g + 2] * rl, o[4 * g + 3] * rl);
        }
    }
}

__global__ __launch_bounds__(512) void qknorm_attn(const unsigned* __restrict__ q, const unsigned* __restrict__ k,
                                                   const unsigned short* __restrict__ v, const unsigned short* __restrict__ tt,
                                                   unsigned* __restrict__ out) {
    __shared__ __attribute__((aligned(16))) char lds[32768];
    const int b = blockIdx.x, bh = (b & 7) * 4 + (b >> 6), qc = (b >> 3) & 7;
    const float t = __uint_as_float((unsigned)tt[bh & (NH - 1)] << 16);
    const size_t ho = (size_t)bh * SEQ * 14;
    slow_path(q + ho, k + ho, v + ho * 2, t, out + ho, qc, lds);
}

extern "C" void kernel_launch(void* const* d_in, const int* in_sizes, int n_in, void* d_out, int out_size, void* d_ws,
                              size_t ws_size, hipStream_t stream) {
    qknorm_attn<<<256, 512, 0, stream>>>((const unsigned*)d_in[0], (const unsigned*)d_in[1], (const unsigned short*)d_in[2],
                                         (const unsigned short*)d_in[3], (unsigned*)d_out);
}
```

```cpp
#include <hip/hip_runtime.h>
#include <hip/hip_bf16.h>

typedef float f32x16 __attribute__((ext_vector_type(16)));
typedef float f32x2 __attribute__((ext_vector_type(2)));
typedef __bf16 bf16x8 __attribute__((ext_vector_type(8)));
typedef __bf16 bf16x2 __attribute__((ext_vector_type(2)));
typedef unsigned u32x4 __attribute__((ext_vector_type(4)));
typedef unsigned u32x2 __attribute__((ext_vector_type(2)));

constexpr int NB = 2, NH = 16, SEQ = 2048, DH = 28;

__device__ __forceinline__ unsigned pk_bf16(float lo, float hi) {
    f32x2 v = {lo, hi};
    return __builtin_bit_cast(unsigned, __builtin_convertvector(v, bf16x2));
}
__device__ __forceinline__ float bf_lo(unsigned w) { return __uint_as_float(w << 16); }
__device__ __forceinline__ float bf_hi(unsigned w) { return __uint_as_float(w & 0xffff0000u); }
__device__ __forceinline__ float rbf(float x) { return bf_lo(pk_bf16(x, 0.f)); }
__device__ __forceinline__ float xhalf_add(float x) { return x + __shfl_xor(x, 32, 64); }
__device__ __forceinline__ float xhalf_max(float x) { return fmaxf(x, __shfl_xor(x, 32, 64)); }

__device__ __forceinline__ void load_norm_half_row(const unsigned* rowp, int h, unsigned (&o)[8]) {
    unsigned w[7];
#pragma unroll
    for (int i = 0; i < 7; ++i) w[i] = rowp[7 * h + i];
    float ss = 0.f;
#pragma unroll
    for (int i = 0; i < 7; ++i) { float a = bf_lo(w[i]), b = bf_hi(w[i]); ss += rbf(a * a); ss += rbf(b * b); }
    ss = rbf(xhalf_add(ss));
    const float den = rbf(rbf(sqrtf(ss)) + 1e-6f);
#pragma unroll
    for (int i = 0; i < 7; ++i) o[i] = pk_bf16(bf_lo(w[i]) / den, bf_hi(w[i]) / den);
    o[7] = 0u;
}

__device__ __forceinline__ void slow_path(const unsigned* q, const unsigned* k, const unsigned short* v, float t,
                                          unsigned* out, int qc, char* lds) {
    const int tid = threadIdx.x, lane = tid & 63, wid = tid >> 6, r = lane & 31, h = lane >> 5;
    u32x4* kl = (u32x4*)lds;
    unsigned short* vl = (unsigned short*)(lds + 16384);
    unsigned qf[8];
    load_norm_half_row(q + (size_t)(qc * 256 + wid * 32 + r) * 14, h, qf);
    const bf16x8 q0 = __builtin_bit_cast(bf16x8, (u32x4){qf[0], qf[1], qf[2], qf[3]});
    const bf16x8 q1 = __builtin_bit_cast(bf16x8, (u32x4){qf[4], qf[5], qf[6], qf[7]});
    f32x16 o = {};
    float m = -INFINITY, l = 0.f;
    for (int c = 0; c < SEQ / 256; ++c) {
        __syncthreads();
        unsigned kf[8];
        load_norm_half_row(k + (size_t)(c * 256 + wid * 32 + r) * 14, h, kf);
        kl[(wid * 2 + 0) * 64 + lane] = (u32x4){kf[0], kf[1], kf[2], kf[3]};
        kl[(wid * 2 + 1) * 64 + lane] = (u32x4){kf[4], kf[5], kf[6], kf[7]};
        for (int i = tid; i < 256 * 14; i += 512) ((unsigned*)vl)[i] = ((const unsigned*)v)[(size_t)c * 256 * 14 + i];
        __syncthreads();
        for (int T = 0; T < 8; ++T) {
            const bf16x8 k0 = __builtin_bit_cast(bf16x8, kl[(T * 2 + 0) * 64 + lane]);
            const bf16x8 k1 = __builtin_bit_cast(bf16x8, kl[(T * 2 + 1) * 64 + lane]);
            f32x16 s = {};
            s = __builtin_amdgcn_mfma_f32_32x32x16_bf16(k0, q0, s, 0, 0, 0);
            s = __builtin_amdgcn_mfma_f32_32x32x16_bf16(k1, q1, s, 0, 0, 0);
            float tm = -INFINITY;
#pragma unroll
            for (int i = 0; i < 16; ++i) { s[i] = rbf(rbf(s[i]) * t); tm = fmaxf(tm, s[i]); }
            tm = xhalf_max(tm);
            const float mn = fmaxf(m, tm);
            const float alpha = __expf(m - mn);
            m = mn;
            float ps = 0.f;
#pragma unroll
            for (int i = 0; i < 16; ++i) { s[i] = __expf(s[i] - mn); ps += s[i]; o[i] *= alpha; }
            l = l * alpha + ps;
#pragma unroll
            for (int st = 0; st < 2; ++st) {
                const bf16x8 p = __builtin_bit_cast(bf16x8, (u32x4){pk_bf16(s[8 * st], s[8 * st + 1]), pk_bf16(s[8 * st + 2], s[8 * st + 3]),
                                                                    pk_bf16(s[8 * st + 4], s[8 * st + 5]), pk_bf16(s[8 * st + 6], s[8 * st + 7])});
                unsigned vw[4];
#pragma unroll
                for (int j = 0; j < 4; ++j) {
                    const int key = T * 32 + 16 * st + 8 * (j >> 1) + 4 * h + 2 * (j & 1);
                    const int d = r < DH ? r : 0;
                    vw[j] = (unsigned)vl[key * DH + d] | ((unsigned)vl[(key + 1) * DH + d] << 16);
                }
                o = __builtin_amdgcn_mfma_f32_32x32x16_bf16(__builtin_bit_cast(bf16x8, (u32x4){vw[0], vw[1], vw[2], vw[3]}), p, o, 0, 0, 0);
            }
        }
    }
    l = xhalf_add(l);
    const float rl = 1.f / l;
    unsigned* orow = out + (size_t)(qc * 256 + wid * 32 + r) * 14;
#pragma unroll
    for (int g = 0; g < 4; ++g) {
        if (g < 3 || h == 0) {
            orow[4 * g + 2 * h] = pk_bf16(o[4 * g] * rl, o[4 * g + 1] * rl);
            orow[4 * g + 2 * h + 1] = pk_bf16(o[4 * g + 2] * rl, o[4 * g + 3] * rl);
        }
    }
}

__global__ __launch_bounds__(512) void qknorm_attn(const unsigned* __restrict__ q, const unsigned* __restrict__ k,
                                                   const unsigned short* __restrict__ v, const unsigned short* __restrict__ tt,
                                                   unsigned* __restrict__ out) {
    __shared__ __attribute__((aligned(16))) char lds[32768];
    const int b = blockIdx.x, bh = (b & 7) * 4 + (b >> 6), qc = (b >> 3) & 7;
    const float t = __uint_as_float((unsigned)tt[bh & (NH - 1)] << 16);
    const size_t ho = (size_t)bh * SEQ * 14;
    unsigned dummy[8];
#pragma unroll
    for (int i = 0; i < 2; ++i) { int ln = threadIdx.x + i * 512; if (ln >= 896) ln = 895;
        asm volatile("global_load_dword %0, %1, off" : "=v"(dummy[i]) : "v"(k + ho + ln * 32) : "memory");
        asm volatile("global_load_dword %0, %1, off" : "=v"(dummy[2 + i]) : "v"((const unsigned*)v + ho + ln * 32) : "memory"); }
    asm volatile("s_waitcnt vmcnt(0)" : "+v"(dummy[0]), "+v"(dummy[1]), "+v"(dummy[2]), "+v"(dummy[3]) :: "memory");
    slow_path(q + ho, k + ho, v + ho * 2, t, out + ho, qc, lds);
}

extern "C" void kernel_launch(void* const* d_in, const int* in_sizes, int n_in, void* d_out, int out_size, void* d_ws,
                              size_t ws_size, hipStream_t stream) {
    qknorm_attn<<<256, 512, 0, stream>>>((const unsigned*)d_in[0], (const unsigned*)d_in[1], (const unsigned short*)d_in[2],
                                         (const unsigned short*)d_in[3], (unsigned*)d_out);
}
```

```cpp
#include <hip/hip_runtime.h>
#include <hip/hip_bf16.h>

typedef float f32x16 __attribute__((ext_vector_type(16)));
typedef float f32x2 __attribute__((ext_vector_type(2)));
typedef __bf16 bf16x8 __attribute__((ext_vector_type(8)));
typedef __bf16 bf16x2 __attribute__((ext_vector_type(2)));
typedef unsigned u32x4 __attribute__((ext_vector_type(4)));
typedef unsigned u32x2 __attribute__((ext_vector_type(2)));

constexpr int NB = 2, NH = 16, SEQ = 2048, DH = 28;

__device__ __forceinline__ unsigned pk_bf16(float lo, float hi) {
    f32x2 v = {lo, hi};
    return __builtin_bit_cast(unsigned, __builtin_convertvector(v, bf16x2));
}
__device__ __forceinline__ float bf_lo(unsigned w) { return __uint_as_float(w << 16); }
__device__ __forceinline__ float bf_hi(unsigned w) { return __uint_as_float(w & 0xffff0000u); }
__device__ __forceinline__ float rbf(float x) { return bf_lo(pk_bf16(x, 0.f)); }
__device__ __forceinline__ float xhalf_add(float x) { return x + __shfl_xor(x, 32, 64); }
__device__ __forceinline__ float xhalf_max(float x) { return fmaxf(x, __shfl_xor(x, 32, 64)); }

__device__ __forceinline__ void load_norm_half_row(const unsigned* rowp, int h, unsigned (&o)[8]) {
    unsigned w[7];
#pragma unroll
    for (int i = 0; i < 7; ++i) w[i] = rowp[7 * h + i];
    float ss = 0.f;
#pragma unroll
    for (int i = 0; i < 7; ++i) { float a = bf_lo(w[i]), b = bf_hi(w[i]); ss += rbf(a * a); ss += rbf(b * b); }
    ss = rbf(xhalf_add(ss));
    const float den = rbf(rbf(sqrtf(ss)) + 1e-6f);
#pragma unroll
    for (int i = 0; i < 7; ++i) o[i] = pk_bf16(bf_lo(w[i]) / den, bf_hi(w[i]) / den);
    o[7] = 0u;
}

__device__ __forceinline__ void slow_path(const unsigned* q, const unsigned* k, const unsigned short* v, float t,
                                          unsigned* out, int qc, char* lds) {
    const int tid = threadIdx.x, lane = tid & 63, wid = tid >> 6, r = lane & 31, h = lane >> 5;
    u32x4* kl = (u32x4*)lds;
    unsigned short* vl = (unsigned short*)(lds + 16384);
    unsigned qf[8];
    load_norm_half_row(q + (size_t)(qc * 256 + wid * 32 + r) * 14, h, qf);
    const bf16x8 q0 = __builtin_bit_cast(bf16x8, (u32x4){qf[0], qf[1], qf[2], qf[3]});
    const bf16x8 q1 = __builtin_bit_cast(bf16x8, (u32x4){qf[4], qf[5], qf[6], qf[7]});
    f32x16 o = {};
    float m = -INFINITY, l = 0.f;
    for (int c = 0; c < SEQ / 256; ++c) {
        __syncthreads();
        unsigned kf[8];
        load_norm_half_row(k + (size_t)(c * 256 + wid * 32 + r) * 14, h, kf);
        kl[(wid * 2 + 0) * 64 + lane] = (u32x4){kf[0], kf[1], kf[2], kf[3]};
        kl[(wid * 2 + 1) * 64 + lane] = (u32x4){kf[4], kf[5], kf[6], kf[7]};
        for (int i = tid; i < 256 * 14; i += 512) ((unsigned*)vl)[i] = ((const unsigned*)v)[(size_t)c * 256 * 14 + i];
        __syncthreads();
        for (int T = 0; T < 8; ++T) {
            const bf16x8 k0 = __builtin_bit_cast(bf16x8, kl[(T * 2 + 0) * 64 + lane]);
            const bf16x8 k1 = __builtin_bit_cast(bf16x8, kl[(T * 2 + 1) * 64 + lane]);
            f32x16 s = {};
            s = __builtin_amdgcn_mfma_f32_32x32x16_bf16(k0, q0, s, 0, 0, 0);
            s = __builtin_amdgcn_mfma_f32_32x32x16_bf16(k1, q1, s, 0, 0, 0);
            float tm = -INFINITY;
#pragma unroll
            for (int i = 0; i < 16; ++i) { s[i] = rbf(rbf(s[i]) * t); tm = fmaxf(tm, s[i]); }
            tm = xhalf_max(tm);
            const float mn = fmaxf(m, tm);
            const float alpha = __expf(m - mn);
            m = mn;
            float ps = 0.f;
#pragma unroll
            for (int i = 0; i < 16; ++i) { s[i] = __expf(s[i] - mn); ps += s[i]; o[i] *= alpha; }
            l = l * alpha + ps;
#pragma unroll
            for (int st = 0; st < 2; ++st) {
                const bf16x8 p = __builtin_bit_cast(bf16x8, (u32x4){pk_bf16(s[8 * st], s[8 * st + 1]), pk_bf16(s[8 * st + 2], s[8 * st + 3]),
                                                                    pk_bf16(s[8 * st + 4], s[8 * st + 5]), pk_bf16(s[8 * st + 6], s[8 * st + 7])});
                unsigned vw[4];
#pragma unroll
                for (int j = 0; j < 4; ++j) {
                    const int key = T * 32 + 16 * st + 8 * (j >> 1) + 4 * h + 2 * (j & 1);
                    const int d = r < DH ? r : 0;
                    vw[j] = (unsigned)vl[key * DH + d] | ((unsigned)vl[(key + 1) * DH + d] << 16);
                }
                o = __builtin_amdgcn_mfma_f32_32x32x16_bf16(__builtin_bit_cast(bf16x8, (u32x4){vw[0], vw[1], vw[2], vw[3]}), p, o, 0, 0, 0);
            }
        }
    }
    l = xhalf_add(l);
    const float rl = 1.f / l;
    unsigned* orow = out + (size_t)(qc * 256 + wid * 32 + r) * 14;
#pragma unroll
    for (int g = 0; g < 4; ++g) {
        if (g < 3 || h == 0) {
            orow[4 * g + 2 * h] = pk_bf16(o[4 * g] * rl, o[4 * g + 1] * rl);
            orow[4 * g + 2 * h + 1] = pk_bf16(o[4 * g + 2] * rl, o[4 * g + 3] * rl);
        }
    }
}

__global__ __launch_bounds__(512) void qknorm_attn(const unsigned* __restrict__ q, const unsigned* __restrict__ k,
                                                   const unsigned short* __restrict__ v, const unsigned short* __restrict__ tt,
                                                   unsigned* __restrict__ out) {
    __shared__ __attribute__((aligned(16))) char lds[160256];
    {

        asm volatile(
        "Lqk_start_%=:\n\t"
        "s_load_dwordx8 s[8:15], %[ka], 0x0\n\t"
        "v_readfirstlane_b32 s36, %[tid]\n\t"
        "v_and_b32 v2, 63, %[tid]\n\t"
        "s_and_b32 s54, %[bid], 7\n\t"
        "s_lshr_b32 s44, %[bid], 6\n\t"
        "s_lshl2_add_u32 s54, s54, s44\n\t"
        "s_lshr_b32 s36, s36, 6\n\t"
        "v_lshlrev_b32 v14, 4, v2\n\t"
        "s_mul_i32 s39, s54, 114688\n\t"
        "s_and_b32 s37, s36, 3\n\t"
        "s_mul_i32 s48, s36, 3584\n\t"
        "s_add_u32 s48, s48, 131072\n\t"
        "s_bfe_u32 s50, %[bid], 0x30003\n\t"
        "s_lshl2_add_u32 s50, s50, s37\n\t"
        "s_mul_i32 s50, s50, 3584\n\t"
        "s_lshr_b32 s44, s37, 1\n\t"
        "s_and_b32 s42, s37, 1\n\t"
        "s_lshl4_add_u32 s4, s44, s42\n\t"
        "s_mul_i32 s4, s4, 3584\n\t"
        "s_mov_b32 s26, 114688\n\t"
        "s_mov_b32 s27, 0x20000\n\t"
        "s_and_b32 s40, s54, 14\n\t"
        "s_lshl_b32 s40, s40, 1\n\t"
        "s_lshr_b32 s38, s36, 2\n\t"
        "s_cmp_eq_u32 s38, 0\n\t"
        "s_cselect_b32 s55, s4, s50\n\t"
        "s_waitcnt lgkmcnt(0)\n\t"
        "s_cselect_b32 s24, s10, s8\n\t"
        "s_cselect_b32 s25, s11, s9\n\t"
        "s_add_u32 s24, s24, s39\n\t"
        "s_addc_u32 s25, s25, 0\n\t"
        "s_cmp_eq_u32 s38, 0\n\t"
        "s_cbranch_scc1 Lqk_kld_%=\n\t"
        "s_mov_b32 m0, s48\n\t"
        "s_nop 0\n\t"
        "buffer_load_dwordx4 v14, s[24:27], s55 offen nt lds\n\t"
        "buffer_load_dwordx4 v14, s[24:27], s55 offen offset:1024 nt lds\n\t"
        "buffer_load_dwordx4 v14, s[24:27], s55 offen offset:2048 nt lds\n\t"
        "s_mov_b32 exec_hi, 0\n\t"
        "buffer_load_dwordx4 v14, s[24:27], s55 offen offset:3072 nt lds\n\t"
        "s_mov_b32 exec_hi, -1\n\t"
        "s_branch Lqk_ldj_%=\n\t"
        "Lqk_kld_%=:\n\t"
        "s_mov_b32 m0, s48\n\t"
        "s_nop 0\n\t"
        "buffer_load_dwordx4 v14, s[24:27], s55 offen lds\n\t"
        "buffer_load_dwordx4 v14, s[24:27], s55 offen offset:1024 lds\n\t"
        "buffer_load_dwordx4 v14, s[24:27], s55 offen offset:2048 lds\n\t"
        "s_mov_b32 exec_hi, 0\n\t"
        "buffer_load_dwordx4 v14, s[24:27], s55 offen offset:3072 lds\n\t"
        "s_mov_b32 exec_hi, -1\n\t"
        "Lqk_ldj_%=:\n\t"
        "s_load_dword s40, s[14:15], s40\n\t"
        "s_load_dwordx2 s[16:17], %[ka], 0x20\n\t"
        "s_add_u32 s28, s12, s39\n\t"
        "s_addc_u32 s29, s13, 0\n\t"
        "s_mov_b32 s30, 114688\n\t"
        "s_mov_b32 s31, 0x20000\n\t"
        "s_lshl_b32 s44, s38, 5\n\t"
        "s_add_u32 s44, s44, s37\n\t"
        "s_mul_i32 s19, s44, 1792\n\t"
        "s_lshl_b32 s45, s38, 14\n\t"
        "s_lshl_b32 s49, s37, 11\n\t"
        "s_add_u32 s49, s49, s45\n\t"
        "s_mov_b32 s52, 0x0fffffff\n\t"
        "s_mov_b32 s53, 0x0fffffff\n\t"
        "v_and_b32 v4, 31, v2\n\t"
        "v_lshrrev_b32 v5, 5, v2\n\t"
        "v_mul_u32_u24 v6, 448, v5\n\t"
        "v_lshl_add_u32 v19, v4, 4, v6\n\t"
        "s_add_u32 s20, s10, s39\n\t"
        "s_addc_u32 s21, s11, 0\n\t"
        "s_mov_b32 s22, 114688\n\t"
        "s_mov_b32 s23, 0x20000\n\t"
        "s_lshl_b32 s46, s36, 12\n\t"
        "v_mad_u32_u24 v23, v2, 56, s48\n\t"
        "v_lshlrev_b32 v17, 11, v5\n\t"
        "v_lshl_add_u32 v17, v4, 4, v17\n\t"
        "s_lshr_b32 s43, s37, 1\n\t"
        "s_and_b32 s42, s37, 1\n\t"
        "s_lshl2_add_u32 s43, s43, s42\n\t"
        "s_lshl_b32 s43, s43, 12\n\t"
        "s_lshl_b32 s42, s38, 11\n\t"
        "s_add_u32 s44, s43, s42\n\t"
        "v_add_u32 v39, s44, v14\n\t"
        "s_xor_b32 s44, s44, 0x800\n\t"
        "v_add_u32 v38, s44, v14\n\t"
        "v_add_u32 v15, s45, v14\n\t"
        "s_add_u32 s44, s46, 65984\n\t"
        "v_lshrrev_b32 v11, 3, v2\n\t"
        "v_lshl_add_u32 v11, v11, 9, s44\n\t"
        "v_and_b32 v12, 7, v2\n\t"
        "v_lshl_add_u32 v18, v12, 3, v11\n\t"
        "v_mov_b32 v40, 0x3f80\n\t"
        "v_mov_b32 v41, 0\n\t"
        "ds_write_b64 v18, v[40:41]\n\t"
        "ds_write_b64 v18, v[40:41] offset:32768\n\t"
        "s_add_u32 s44, s45, 65536\n\t"
        "v_lshl_add_u32 v9, v5, 9, s44\n\t"
        "v_bfe_u32 v11, v2, 2, 2\n\t"
        "v_mad_u32_u24 v11, v11, 56, v9\n\t"
        "v_and_b32 v12, 16, v2\n\t"
        "v_add_u32 v11, v12, v11\n\t"
        "v_and_b32 v12, 2, v2\n\t"
        "v_lshl_add_u32 v11, v12, 2, v11\n\t"
        "v_and_b32 v12, 1, v2\n\t"
        "v_lshl_add_u32 v11, v12, 5, v11\n\t"
        "v_and_b32 v12, 19, v2\n\t"
        "v_cmp_eq_u32 vcc, 19, v12\n\t"
        "v_add_u32 v7, 480, v9\n\t"
        "v_add_u32 v8, 448, v9\n\t"
        "v_add_u32 v12, 224, v11\n\t"
        "v_mov_b32 v26, 0xdddddddd\n\t"
        "v_cndmask_b32 v36, v11, v7, vcc\n\t"
        "v_cndmask_b32 v37, v12, v8, vcc\n\t"
        "v_mov_b64 v[154:155], 0\n\t"
        "v_mov_b64 v[156:157], 0\n\t"
        "v_mov_b32 v27, 0x3fb8aa3b\n\t"
        "s_mul_i32 s5, s38, 57344\n\t"
        "s_mul_i32 s44, s37, 3584\n\t"
        "s_add_u32 s5, s44, s5\n\t"
        "v_mfma_f32_32x32x16_bf16 v[58:73], v[154:157], v[154:157], 0\n\t"
        "s_add_u32 s5, s5, 7168\n\t"
        "v_mfma_f32_32x32x16_bf16 v[74:89], v[154:157], v[154:157], 0\n\t"
        "s_add_u32 m0, s49, 73728\n\t"
        "s_mov_b64 exec, s[52:53]\n\t"
        "buffer_load_dwordx4 v19, s[28:31], s19 offen nt lds\n\t"
        "s_mov_b64 exec, -1\n\t"
        "s_add_u32 m0, s49, 73856\n\t"
        "s_mov_b64 exec, s[52:53]\n\t"
        "buffer_load_dwordx4 v19, s[28:31], s19 offen offset:896 nt lds\n\t"
        "s_mov_b64 exec, -1\n\t"
        "s_cmp_eq_u32 s38, 0\n\t"
        "s_cselect_b64 vcc, -1, 0\n\t"
        "s_cselect_b32 s44, 8192, 0\n\t"
        "s_add_u32 s44, s44, s43\n\t"
        "v_add_u32 v5, s44, v17\n\t"
        "s_and_b32 s41, s54, 1\n\t"
        "s_lshl_b32 s41, s41, 4\n\t"
        "s_add_u32 s6, s5, 14336\n\t"
        "s_add_u32 s7, s5, 28672\n\t"
        "s_add_u32 s18, s4, 50176\n\t"
        "s_add_u32 s51, s19, 50176\n\t"
        "v_add_u32 v16, s46, v17\n\t"
        "s_cmp_lg_u32 s38, 0\n\t"
        "s_cselect_b32 s42, s48, s43\n\t"
        "v_add_u32 v33, s42, v17\n\t"
        "v_add_u32 v21, s46, v14\n\t"
        "s_xor_b32 s47, s46, 0x4000\n\t"
        "v_add_u32 v22, s47, v14\n\t"
        "v_add_u32 v25, s48, v14\n\t"
        "s_mul_i32 s44, s38, 1792\n\t"
        "s_add_u32 s44, s44, s50\n\t"
        "v_add_u32 v20, s44, v14\n\t"
        "v_and_b32 v6, 31, v2\n\t"
        "v_mad_u32_u24 v6, v6, 56, s48\n\t"
        "v_and_b32 v7, 32, v2\n\t"
        "v_add_u32 v24, v7, v6\n\t"
        "s_mov_b32 s58, 114688\n\t"
        "s_mov_b32 s59, 0x20000\n\t"
        "s_waitcnt vmcnt(2)\n\t"
        "ds_read_b64 v[178:179], v23\n\t"
        "ds_read_b64 v[180:181], v23 offset:8\n\t"
        "ds_read_b64 v[182:183], v23 offset:16\n\t"
        "ds_read_b64 v[184:185], v23 offset:24\n\t"
        "ds_read_b64 v[186:187], v23 offset:32\n\t"
        "ds_read_b64 v[188:189], v23 offset:40\n\t"
        "ds_read_b64 v[190:191], v23 offset:48\n\t"
        "s_waitcnt lgkmcnt(0)\n\t"
        "s_lshr_b32 s40, s40, s41\n\t"
        "v_dot2_f32_bf16 v208, v178, v178, 0\n\t"
        "v_dot2_f32_bf16 v209, v179, v179, 0\n\t"
        "s_and_b32 s44, s40, 0x7fff\n\t"
        "v_dot2_f32_bf16 v208, v180, v180, v208\n\t"
        "v_dot2_f32_bf16 v209, v181, v181, v209\n\t"
        "s_cmp_gt_u32 s44, 0x40e0\n\t"
        "s_cbranch_scc1 Lqk_slow_%=\n\t"
        "v_dot2_f32_bf16 v208, v182, v182, v208\n\t"
        "v_dot2_f32_bf16 v209, v183, v183, v209\n\t"
        "s_lshl_b32 s40, s40, 16\n\t"
        "v_dot2_f32_bf16 v208, v184, v184, v208\n\t"
        "v_dot2_f32_bf16 v209, v185, v185, v209\n\t"
        "v_dot2_f32_bf16 v208, v186, v186, v208\n\t"
        "v_dot2_f32_bf16 v209, v187, v187, v209\n\t"
        "v_dot2_f32_bf16 v208, v188, v188, v208\n\t"
        "v_dot2_f32_bf16 v209, v189, v189, v209\n\t"
        "v_dot2_f32_bf16 v208, v190, v190, v208\n\t"
        "v_dot2_f32_bf16 v209, v191, v191, v209\n\t"
        "v_mul_f32 v27, s40, v27\n\t"
        "v_mul_f32_e64 v4, -0.5, |v27|\n\t"
        "v_cvt_pk_bf16_f32 v4, v4, 0\n\t"
        "v_add_f32 v208, v208, v209\n\t"
        "v_sqrt_f32 v208, v208\n\t"
        "v_cndmask_b32 v199, v4, v40, vcc\n\t"
        "v_add_f32 v208, 0x358637bd, v208\n\t"
        "v_rcp_f32 v208, v208\n\t"
        "v_cndmask_b32_e64 v27, v27, 1.0, vcc\n\t"
        "v_mul_f32 v208, v27, v208\n\t"
        "v_cvt_pk_bf16_f32 v212, v208, 0\n\t"
        "v_cvt_pk_bf16_f32 v213, 0, v208\n\t"
        "v_cndmask_b32 v207, v4, v40, vcc\n\t"
        "v_dot2_f32_bf16 v214, v178, v212, 0\n\t"
        "v_dot2_f32_bf16 v215, v178, v213, 0\n\t"
        "v_dot2_f32_bf16 v216, v179, v212, 0\n\t"
        "v_dot2_f32_bf16 v217, v179, v213, 0\n\t"
        "v_dot2_f32_bf16 v218, v180, v212, 0\n\t"
        "v_dot2_f32_bf16 v219, v180, v213, 0\n\t"
        "v_cvt_pk_bf16_f32 v192, v214, v215\n\t"
        "v_dot2_f32_bf16 v220, v181, v212, 0\n\t"
        "v_dot2_f32_bf16 v221, v181, v213, 0\n\t"
        "v_cvt_pk_bf16_f32 v193, v216, v217\n\t"
        "v_dot2_f32_bf16 v214, v182, v212, 0\n\t"
        "v_dot2_f32_bf16 v215, v182, v213, 0\n\t"
        "v_cvt_pk_bf16_f32 v194, v218, v219\n\t"
        "v_dot2_f32_bf16 v216, v183, v212, 0\n\t"
        "v_dot2_f32_bf16 v217, v183, v213, 0\n\t"
        "v_cvt_pk_bf16_f32 v195, v220, v221\n\t"
        "ds_write_b128 v5, v[192:195]\n\t"
        "v_dot2_f32_bf16 v218, v184, v212, 0\n\t"
        "v_dot2_f32_bf16 v219, v184, v213, 0\n\t"
        "v_cvt_pk_bf16_f32 v196, v214, v215\n\t"
        "v_dot2_f32_bf16 v220, v185, v212, 0\n\t"
        "v_dot2_f32_bf16 v221, v185, v213, 0\n\t"
        "v_cvt_pk_bf16_f32 v197, v216, v217\n\t"
        "v_dot2_f32_bf16 v214, v186, v212, 0\n\t"
        "v_dot2_f32_bf16 v215, v186, v213, 0\n\t"
        "v_cvt_pk_bf16_f32 v198, v218, v219\n\t"
        "ds_write_b128 v5, v[196:199] offset:1024\n\t"
        "v_dot2_f32_bf16 v216, v187, v212, 0\n\t"
        "v_dot2_f32_bf16 v217, v187, v213, 0\n\t"
        "v_cvt_pk_bf16_f32 v200, v220, v221\n\t"
        "v_dot2_f32_bf16 v218, v188, v212, 0\n\t"
        "v_dot2_f32_bf16 v219, v188, v213, 0\n\t"
        "v_cvt_pk_bf16_f32 v201, v214, v215\n\t"
        "v_dot2_f32_bf16 v220, v189, v212, 0\n\t"
        "v_dot2_f32_bf16 v221, v189, v213, 0\n\t"
        "v_cvt_pk_bf16_f32 v202, v216, v217\n\t"
        "v_dot2_f32_bf16 v214, v190, v212, 0\n\t"
        "v_dot2_f32_bf16 v215, v190, v213, 0\n\t"
        "v_cvt_pk_bf16_f32 v203, v218, v219\n\t"
        "ds_write_b128 v5, v[200:203] offset:512\n\t"
        "v_dot2_f32_bf16 v216, v191, v212, 0\n\t"
        "v_dot2_f32_bf16 v217, v191, v213, 0\n\t"
        "v_cvt_pk_bf16_f32 v204, v220, v221\n\t"
        "v_cvt_pk_bf16_f32 v205, v214, v215\n\t"
        "s_nop 0\n\t"
        "v_cvt_pk_bf16_f32 v206, v216, v217\n\t"
        "ds_write_b128 v5, v[204:207] offset:1536\n\t"
        "s_waitcnt vmcnt(0)\n\t"
        "s_waitcnt lgkmcnt(0)\n\t"
        "s_barrier\n\t"
        "s_setprio 3\n\t"
        "ds_read_b128 v[154:157], v15 offset:8192\n\t"
        "ds_read_b128 v[42:45], v38\n\t"
        "ds_read_b128 v[158:161], v15 offset:9216\n\t"
        "ds_read_b128 v[46:49], v38 offset:1024\n\t"
        "ds_read_b128 v[50:53], v39\n\t"
        "ds_read_b128 v[54:57], v39 offset:1024\n\t"
        "ds_read_b64_tr_b16 v[162:163], v36 offset:8192\n\t"
        "ds_read_b64_tr_b16 v[164:165], v37 offset:8192\n\t"
        "ds_read_b64_tr_b16 v[170:171], v36 offset:9216\n\t"
        "ds_read_b64_tr_b16 v[172:173], v37 offset:9216\n\t"
        "s_mov_b32 m0, s48\n\t"
        "s_nop 0\n\t"
        "buffer_load_dwordx4 v14, s[20:23], s5 offen lds\n\t"
        "v_mov_b32 v199, 0x3f80\n\t"
        "v_mov_b32 v207, 0x3f80\n\t"
        "s_waitcnt lgkmcnt(8)\n\t"
        "v_mfma_f32_32x32x16_bf16 v[90:105], v[154:157], v[42:45], 0\n\t"
        "buffer_load_dwordx4 v14, s[20:23], s5 offen offset:1024 lds\n\t"
        "s_waitcnt lgkmcnt(6)\n\t"
        "v_mfma_f32_32x32x16_bf16 v[90:105], v[158:161], v[46:49], v[90:105]\n\t"
        "buffer_load_dwordx4 v14, s[20:23], s5 offen offset:2048 lds\n\t"
        "s_waitcnt lgkmcnt(4)\n\t"
        "v_mfma_f32_32x32x16_bf16 v[106:121], v[154:157], v[50:53], 0\n\t"
        "ds_read_b128 v[154:157], v15 offset:10240\n\t"
        "v_mfma_f32_32x32x16_bf16 v[106:121], v[158:161], v[54:57], v[106:121]\n\t"
        "ds_read_b128 v[158:161], v15 offset:11264\n\t"
        "ds_read_b64_tr_b16 v[166:167], v36 offset:10240\n\t"
        "ds_read_b64_tr_b16 v[168:169], v37 offset:10240\n\t"
        "s_mov_b32 exec_hi, 0\n\t"
        "buffer_load_dwordx4 v14, s[20:23], s5 offen offset:3072 lds\n\t"
        "s_mov_b32 exec_hi, -1\n\t"
        "s_cmp_lg_u32 s38, 0\n\t"
        "s_cselect_b64 vcc, -1, 0\n\t"
        "v_exp_f32 v90, v90\n\t"
        "v_exp_f32 v91, v91\n\t"
        "s_waitcnt lgkmcnt(2)\n\t"
        "v_exp_f32 v92, v92\n\t"
        "v_exp_f32 v93, v93\n\t"
        "v_exp_f32 v94, v94\n\t"
        "v_exp_f32 v95, v95\n\t"
        "ds_read_b64_tr_b16 v[174:175], v36 offset:11264\n\t"
        "ds_read_b64_tr_b16 v[176:177], v37 offset:11264\n\t"
        "v_exp_f32 v96, v96\n\t"
        "v_exp_f32 v97, v97\n\t"
        "v_exp_f32 v106, v106\n\t"
        "v_exp_f32 v107, v107\n\t"
        "v_mfma_f32_32x32x16_bf16 v[122:137], v[154:157], v[42:45], 0\n\t"
        "s_add_u32 m0, s46, 98304\n\t"
        "s_mov_b64 exec, s[52:53]\n\t"
        "buffer_load_dwordx4 v19, s[28:31], s5 offen lds\n\t"
        "s_mov_b64 exec, -1\n\t"
        "v_exp_f32 v108, v108\n\t"
        "v_exp_f32 v109, v109\n\t"
        "v_exp_f32 v110, v110\n\t"
        "v_exp_f32 v111, v111\n\t"
        "v_mfma_f32_32x32x16_bf16 v[138:153], v[154:157], v[50:53], 0\n\t"
        "ds_read_b128 v[154:157], v15 offset:12288\n\t"
        "v_exp_f32 v112, v112\n\t"
        "v_exp_f32 v113, v113\n\t"
        "v_exp_f32 v98, v98\n\t"
        "v_exp_f32 v99, v99\n\t"
        "v_mfma_f32_32x32x16_bf16 v[122:137], v[158:161], v[46:49], v[122:137]\n\t"
        "s_add_u32 m0, s46, 98432\n\t"
        "s_mov_b64 exec, s[52:53]\n\t"
        "buffer_load_dwordx4 v19, s[28:31], s5 offen offset:896 lds\n\t"
        "s_mov_b64 exec, -1\n\t"
        "v_exp_f32 v100, v100\n\t"
        "v_exp_f32 v101, v101\n\t"
        "v_exp_f32 v102, v102\n\t"
        "v_exp_f32 v103, v103\n\t"
        "v_mfma_f32_32x32x16_bf16 v[138:153], v[158:161], v[54:57], v[138:153]\n\t"
        "ds_read_b128 v[158:161], v15 offset:13312\n\t"
        "v_exp_f32 v104, v104\n\t"
        "v_exp_f32 v105, v105\n\t"
        "v_exp_f32 v114, v114\n\t"
        "v_exp_f32 v115, v115\n\t"
        "v_smfmac_f32_32x32x32_bf16 v[58:73], v[162:165], v[90:97], v26\n\t"
        "s_add_u32 m0, s46, 98560\n\t"
        "s_mov_b64 exec, s[52:53]\n\t"
        "buffer_load_dwordx4 v19, s[28:31], s5 offen offset:1792 lds\n\t"
        "s_mov_b64 exec, -1\n\t"
        "v_exp_f32 v116, v116\n\t"
        "v_exp_f32 v117, v117\n\t"
        "v_exp_f32 v118, v118\n\t"
        "v_exp_f32 v119, v119\n\t"
        "v_smfmac_f32_32x32x32_bf16 v[74:89], v[162:165], v[106:113], v26\n\t"
        "ds_read_b64_tr_b16 v[162:163], v36 offset:12288\n\t"
        "ds_read_b64_tr_b16 v[164:165], v37 offset:12288\n\t"
        "v_exp_f32 v120, v120\n\t"
        "v_exp_f32 v121, v121\n\t"
        "v_exp_f32 v122, v122\n\t"
        "v_exp_f32 v123, v123\n\t"
        "v_smfmac_f32_32x32x32_bf16 v[58:73], v[170:173], v[98:105], v26\n\t"
        "s_waitcnt lgkmcnt(2)\n\t"
        "s_setprio 2\n\t"
        "v_exp_f32 v124, v124\n\t"
        "v_exp_f32 v125, v125\n\t"
        "v_exp_f32 v126, v126\n\t"
        "v_exp_f32 v127, v127\n\t"
        "v_smfmac_f32_32x32x32_bf16 v[74:89], v[170:173], v[114:121], v26\n\t"
        "ds_read_b64_tr_b16 v[170:171], v36 offset:13312\n\t"
        "ds_read_b64_tr_b16 v[172:173], v37 offset:13312\n\t"
        "v_exp_f32 v128, v128\n\t"
        "v_exp_f32 v129, v129\n\t"
        "v_exp_f32 v138, v138\n\t"
        "v_exp_f32 v139, v139\n\t"
        "v_mfma_f32_32x32x16_bf16 v[90:105], v[154:157], v[42:45], 0\n\t"
        "s_add_u32 m0, s46, 98688\n\t"
        "s_mov_b64 exec, s[52:53]\n\t"
        "buffer_load_dwordx4 v19, s[28:31], s5 offen offset:2688 lds\n\t"
        "s_mov_b64 exec, -1\n\t"
        "v_exp_f32 v140, v140\n\t"
        "v_exp_f32 v141, v141\n\t"
        "v_exp_f32 v142, v142\n\t"
        "v_exp_f32 v143, v143\n\t"
        "v_mfma_f32_32x32x16_bf16 v[106:121], v[154:157], v[50:53], 0\n\t"
        "ds_read_b128 v[154:157], v15 offset:14336\n\t"
        "v_exp_f32 v144, v144\n\t"
        "v_exp_f32 v145, v145\n\t"
        "v_exp_f32 v130, v130\n\t"
        "v_exp_f32 v131, v131\n\t"
        "v_mfma_f32_32x32x16_bf16 v[90:105], v[158:161], v[46:49], v[90:105]\n\t"
        "s_waitcnt vmcnt(4)\n\t"
        "ds_read_b64 v[178:179], v23\n\t"
        "ds_read_b64 v[180:181], v23 offset:8\n\t"
        "ds_read_b64 v[182:183], v23 offset:16\n\t"
        "v_exp_f32 v132, v132\n\t"
        "v_exp_f32 v133, v133\n\t"
        "v_exp_f32 v134, v134\n\t"
        "v_exp_f32 v135, v135\n\t"
        "v_mfma_f32_32x32x16_bf16 v[106:121], v[158:161], v[54:57], v[106:121]\n\t"
        "ds_read_b128 v[158:161], v15 offset:15360\n\t"
        "v_exp_f32 v136, v136\n\t"
        "v_exp_f32 v137, v137\n\t"
        "v_exp_f32 v146, v146\n\t"
        "v_exp_f32 v147, v147\n\t"
        "v_smfmac_f32_32x32x32_bf16 v[58:73], v[166:169], v[122:129], v26\n\t"
        "ds_read_b64 v[184:185], v23 offset:24\n\t"
        "ds_read_b64 v[186:187], v23 offset:32\n\t"
        "ds_read_b64 v[188:189], v23 offset:40\n\t"
        "ds_read_b64 v[190:191], v23 offset:48\n\t"
        "v_exp_f32 v148, v148\n\t"
        "v_exp_f32 v149, v149\n\t"
        "v_exp_f32 v150, v150\n\t"
        "v_exp_f32 v151, v151\n\t"
        "v_smfmac_f32_32x32x32_bf16 v[74:89], v[166:169], v[138:145], v26\n\t"
        "ds_read_b64_tr_b16 v[166:167], v36 offset:14336\n\t"
        "ds_read_b64_tr_b16 v[168:169], v37 offset:14336\n\t"
        "v_exp_f32 v152, v152\n\t"
        "v_exp_f32 v153, v153\n\t"
        "v_exp_f32 v90, v90\n\t"
        "v_exp_f32 v91, v91\n\t"
        "v_smfmac_f32_32x32x32_bf16 v[58:73], v[174:177], v[130:137], v26\n\t"
        "s_waitcnt lgkmcnt(6)\n\t"
        "s_setprio 1\n\t"
        "s_waitcnt lgkmcnt(2)\n\t"
        "s_setprio 3\n\t"
        "v_dot2_f32_bf16 v208, v178, v178, 0\n\t"
        "v_dot2_f32_bf16 v209, v179, v179, 0\n\t"
        "v_dot2_f32_bf16 v208, v180, v180, v208\n\t"
        "v_dot2_f32_bf16 v209, v181, v181, v209\n\t"
        "v_dot2_f32_bf16 v208, v182, v182, v208\n\t"
        "v_dot2_f32_bf16 v209, v183, v183, v209\n\t"
        "v_dot2_f32_bf16 v208, v184, v184, v208\n\t"
        "v_dot2_f32_bf16 v209, v185, v185, v209\n\t"
        "v_dot2_f32_bf16 v208, v186, v186, v208\n\t"
        "v_dot2_f32_bf16 v209, v187, v187, v209\n\t"
        "v_dot2_f32_bf16 v208, v188, v188, v208\n\t"
        "v_dot2_f32_bf16 v209, v189, v189, v209\n\t"
        "v_dot2_f32_bf16 v208, v190, v190, v208\n\t"
        "v_dot2_f32_bf16 v209, v191, v191, v209\n\t"
        "s_nop 2\n\t"
        "v_add_f32 v208, v208, v209\n\t"
        "v_sqrt_f32 v208, v208\n\t"
        "s_nop 0\n\t"
        "v_add_f32 v208, 0x358637bd, v208\n\t"
        "v_rcp_f32 v208, v208\n\t"
        "s_nop 0\n\t"
        "v_cvt_pk_bf16_f32 v212, v208, 0\n\t"
        "v_cvt_pk_bf16_f32 v213, 0, v208\n\t"
        "v_dot2_f32_bf16 v214, v178, v212, 0\n\t"
        "v_dot2_f32_bf16 v215, v178, v213, 0\n\t"
        "v_dot2_f32_bf16 v216, v179, v212, 0\n\t"
        "v_dot2_f32_bf16 v217, v179, v213, 0\n\t"
        "v_dot2_f32_bf16 v218, v180, v212, 0\n\t"
        "v_dot2_f32_bf16 v219, v180, v213, 0\n\t"
        "v_cvt_pk_bf16_f32 v192, v214, v215\n\t"
        "v_dot2_f32_bf16 v220, v181, v212, 0\n\t"
        "v_dot2_f32_bf16 v221, v181, v213, 0\n\t"
        "v_cvt_pk_bf16_f32 v193, v216, v217\n\t"
        "v_dot2_f32_bf16 v214, v182, v212, 0\n\t"
        "v_dot2_f32_bf16 v215, v182, v213, 0\n\t"
        "v_cvt_pk_bf16_f32 v194, v218, v219\n\t"
        "v_dot2_f32_bf16 v216, v183, v212, 0\n\t"
        "v_dot2_f32_bf16 v217, v183, v213, 0\n\t"
        "v_cvt_pk_bf16_f32 v195, v220, v221\n\t"
        "v_dot2_f32_bf16 v218, v184, v212, 0\n\t"
        "v_dot2_f32_bf16 v219, v184, v213, 0\n\t"
        "v_cvt_pk_bf16_f32 v196, v214, v215\n\t"
        "v_dot2_f32_bf16 v220, v185, v212, 0\n\t"
        "v_dot2_f32_bf16 v221, v185, v213, 0\n\t"
        "v_cvt_pk_bf16_f32 v197, v216, v217\n\t"
        "v_dot2_f32_bf16 v214, v186, v212, 0\n\t"
        "v_dot2_f32_bf16 v215, v186, v213, 0\n\t"
        "v_cvt_pk_bf16_f32 v198, v218, v219\n\t"
        "v_dot2_f32_bf16 v216, v187, v212, 0\n\t"
        "v_dot2_f32_bf16 v217, v187, v213, 0\n\t"
        "v_cvt_pk_bf16_f32 v200, v220, v221\n\t"
        "v_dot2_f32_bf16 v218, v188, v212, 0\n\t"
        "v_dot2_f32_bf16 v219, v188, v213, 0\n\t"
        "v_cvt_pk_bf16_f32 v201, v214, v215\n\t"
        "v_dot2_f32_bf16 v220, v189, v212, 0\n\t"
        "v_dot2_f32_bf16 v221, v189, v213, 0\n\t"
        "v_cvt_pk_bf16_f32 v202, v216, v217\n\t"
        "v_dot2_f32_bf16 v214, v190, v212, 0\n\t"
        "v_dot2_f32_bf16 v215, v190, v213, 0\n\t"
        "v_cvt_pk_bf16_f32 v203, v218, v219\n\t"
        "v_dot2_f32_bf16 v216, v191, v212, 0\n\t"
        "v_dot2_f32_bf16 v217, v191, v213, 0\n\t"
        "v_cvt_pk_bf16_f32 v204, v220, v221\n\t"
        "v_cvt_pk_bf16_f32 v205, v214, v215\n\t"
        "s_nop 0\n\t"
        "v_cvt_pk_bf16_f32 v206, v216, v217\n\t"
        "s_setprio 1\n\t"
        "v_exp_f32 v92, v92\n\t"
        "v_exp_f32 v93, v93\n\t"
        "v_exp_f32 v94, v94\n\t"
        "v_exp_f32 v95, v95\n\t"
        "v_smfmac_f32_32x32x32_bf16 v[74:89], v[174:177], v[146:153], v26\n\t"
        "ds_read_b64_tr_b16 v[174:175], v36 offset:15360\n\t"
        "ds_read_b64_tr_b16 v[176:177], v37 offset:15360\n\t"
        "ds_write_b128 v16, v[192:195] offset:32768\n\t"
        "v_exp_f32 v96, v96\n\t"
        "v_exp_f32 v97, v97\n\t"
        "v_exp_f32 v106, v106\n\t"
        "v_exp_f32 v107, v107\n\t"
        "v_mfma_f32_32x32x16_bf16 v[122:137], v[154:157], v[42:45], 0\n\t"
        "ds_write_b128 v16, v[196:199] offset:33792\n\t"
        "v_exp_f32 v108, v108\n\t"
        "v_exp_f32 v109, v109\n\t"
        "v_exp_f32 v110, v110\n\t"
        "v_exp_f32 v111, v111\n\t"
        "v_mfma_f32_32x32x16_bf16 v[138:153], v[154:157], v[50:53], 0\n\t"
        "ds_write_b128 v16, v[200:203] offset:33280\n\t"
        "v_exp_f32 v112, v112\n\t"
        "v_exp_f32 v113, v113\n\t"
        "v_exp_f32 v98, v98\n\t"
        "v_exp_f32 v99, v99\n\t"
        "v_mfma_f32_32x32x16_bf16 v[122:137], v[158:161], v[46:49], v[122:137]\n\t"
        "ds_write_b128 v16, v[204:207] offset:34304\n\t"
        "s_waitcnt vmcnt(0)\n\t"
        "v_exp_f32 v100, v100\n\t"
        "v_exp_f32 v101, v101\n\t"
        "v_exp_f32 v102, v102\n\t"
        "v_exp_f32 v103, v103\n\t"
        "v_mfma_f32_32x32x16_bf16 v[138:153], v[158:161], v[54:57], v[138:153]\n\t"
        "s_waitcnt lgkmcnt(0)\n\t"
        "s_barrier\n\t"
        "ds_read_b128 v[154:157], v15 offset:32768\n\t"
        "ds_read_b128 v[158:161], v15 offset:33792\n\t"
        "v_exp_f32 v104, v104\n\t"
        "v_exp_f32 v105, v105\n\t"
        "v_exp_f32 v114, v114\n\t"
        "v_exp_f32 v115, v115\n\t"
        "v_smfmac_f32_32x32x32_bf16 v[58:73], v[162:165], v[90:97], v26\n\t"
        "s_mov_b32 m0, s48\n\t"
        "s_nop 0\n\t"
        "buffer_load_dwordx4 v14, s[20:23], s6 offen lds\n\t"
        "v_exp_f32 v116, v116\n\t"
        "v_exp_f32 v117, v117\n\t"
        "v_exp_f32 v118, v118\n\t"
        "v_exp_f32 v119, v119\n\t"
        "v_smfmac_f32_32x32x32_bf16 v[74:89], v[162:165], v[106:113], v26\n\t"
        "ds_read_b64_tr_b16 v[162:163], v36 offset:32768\n\t"
        "ds_read_b64_tr_b16 v[164:165], v37 offset:32768\n\t"
        "v_exp_f32 v120, v120\n\t"
        "v_exp_f32 v121, v121\n\t"
        "v_exp_f32 v122, v122\n\t"
        "v_exp_f32 v123, v123\n\t"
        "v_smfmac_f32_32x32x32_bf16 v[58:73], v[170:173], v[98:105], v26\n\t"
        "s_waitcnt lgkmcnt(2)\n\t"
        "s_setprio 3\n\t"
        "v_exp_f32 v124, v124\n\t"
        "v_exp_f32 v125, v125\n\t"
        "v_exp_f32 v126, v126\n\t"
        "v_exp_f32 v127, v127\n\t"
        "v_smfmac_f32_32x32x32_bf16 v[74:89], v[170:173], v[114:121], v26\n\t"
        "ds_read_b64_tr_b16 v[170:171], v36 offset:33792\n\t"
        "ds_read_b64_tr_b16 v[172:173], v37 offset:33792\n\t"
        "v_exp_f32 v128, v128\n\t"
        "v_exp_f32 v129, v129\n\t"
        "v_exp_f32 v138, v138\n\t"
        "v_exp_f32 v139, v139\n\t"
        "v_mfma_f32_32x32x16_bf16 v[90:105], v[154:157], v[42:45], 0\n\t"
        "buffer_load_dwordx4 v14, s[20:23], s6 offen offset:1024 lds\n\t"
        "v_exp_f32 v140, v140\n\t"
        "v_exp_f32 v141, v141\n\t"
        "v_exp_f32 v142, v142\n\t"
        "v_exp_f32 v143, v143\n\t"
        "v_mfma_f32_32x32x16_bf16 v[106:121], v[154:157], v[50:53], 0\n\t"
        "ds_read_b128 v[154:157], v15 offset:34816\n\t"
        "v_exp_f32 v144, v144\n\t"
        "v_exp_f32 v145, v145\n\t"
        "v_exp_f32 v130, v130\n\t"
        "v_exp_f32 v131, v131\n\t"
        "v_mfma_f32_32x32x16_bf16 v[90:105], v[158:161], v[46:49], v[90:105]\n\t"
        "buffer_load_dwordx4 v14, s[20:23], s6 offen offset:2048 lds\n\t"
        "v_exp_f32 v132, v132\n\t"
        "v_exp_f32 v133, v133\n\t"
        "v_exp_f32 v134, v134\n\t"
        "v_exp_f32 v135, v135\n\t"
        "v_mfma_f32_32x32x16_bf16 v[106:121], v[158:161], v[54:57], v[106:121]\n\t"
        "ds_read_b128 v[158:161], v15 offset:35840\n\t"
        "v_exp_f32 v136, v136\n\t"
        "v_exp_f32 v137, v137\n\t"
        "v_exp_f32 v146, v146\n\t"
        "v_exp_f32 v147, v147\n\t"
        "v_smfmac_f32_32x32x32_bf16 v[58:73], v[166:169], v[122:129], v26\n\t"
        "s_mov_b32 exec_hi, 0\n\t"
        "buffer_load_dwordx4 v14, s[20:23], s6 offen offset:3072 lds\n\t"
        "s_mov_b32 exec_hi, -1\n\t"
        "v_exp_f32 v148, v148\n\t"
        "v_exp_f32 v149, v149\n\t"
        "v_exp_f32 v150, v150\n\t"
        "v_exp_f32 v151, v151\n\t"
        "v_smfmac_f32_32x32x32_bf16 v[74:89], v[166:169], v[138:145], v26\n\t"
        "ds_read_b64_tr_b16 v[166:167], v36 offset:34816\n\t"
        "ds_read_b64_tr_b16 v[168:169], v37 offset:34816\n\t"
        "v_exp_f32 v152, v152\n\t"
        "v_exp_f32 v153, v153\n\t"
        "v_exp_f32 v90, v90\n\t"
        "v_exp_f32 v91, v91\n\t"
        "v_smfmac_f32_32x32x32_bf16 v[58:73], v[174:177], v[130:137], v26\n\t"
        "s_waitcnt lgkmcnt(2)\n\t"
        "v_exp_f32 v92, v92\n\t"
        "v_exp_f32 v93, v93\n\t"
        "v_exp_f32 v94, v94\n\t"
        "v_exp_f32 v95, v95\n\t"
        "v_smfmac_f32_32x32x32_bf16 v[74:89], v[174:177], v[146:153], v26\n\t"
        "ds_read_b64_tr_b16 v[174:175], v36 offset:35840\n\t"
        "ds_read_b64_tr_b16 v[176:177], v37 offset:35840\n\t"
        "v_exp_f32 v96, v96\n\t"
        "v_exp_f32 v97, v97\n\t"
        "v_exp_f32 v106, v106\n\t"
        "v_exp_f32 v107, v107\n\t"
        "v_mfma_f32_32x32x16_bf16 v[122:137], v[154:157], v[42:45], 0\n\t"
        "s_add_u32 m0, s46, 65536\n\t"
        "s_mov_b64 exec, s[52:53]\n\t"
        "buffer_load_dwordx4 v19, s[28:31], s6 offen lds\n\t"
        "s_mov_b64 exec, -1\n\t"
        "v_exp_f32 v108, v108\n\t"
        "v_exp_f32 v109, v109\n\t"
        "v_exp_f32 v110, v110\n\t"
        "v_exp_f32 v111, v111\n\t"
        "v_mfma_f32_32x32x16_bf16 v[138:153], v[154:157], v[50:53], 0\n\t"
        "ds_read_b128 v[154:157], v15 offset:36864\n\t"
        "v_exp_f32 v112, v112\n\t"
        "v_exp_f32 v113, v113\n\t"
        "v_exp_f32 v98, v98\n\t"
        "v_exp_f32 v99, v99\n\t"
        "v_mfma_f32_32x32x16_bf16 v[122:137], v[158:161], v[46:49], v[122:137]\n\t"
        "v_exp_f32 v100, v100\n\t"
        "v_exp_f32 v101, v101\n\t"
        "v_exp_f32 v102, v102\n\t"
        "v_exp_f32 v103, v103\n\t"
        "v_mfma_f32_32x32x16_bf16 v[138:153], v[158:161], v[54:57], v[138:153]\n\t"
        "ds_read_b128 v[158:161], v15 offset:37888\n\t"
        "v_exp_f32 v104, v104\n\t"
        "v_exp_f32 v105, v105\n\t"
        "v_exp_f32 v114, v114\n\t"
        "v_exp_f32 v115, v115\n\t"
        "v_smfmac_f32_32x32x32_bf16 v[58:73], v[162:165], v[90:97], v26\n\t"
        "s_add_u32 m0, s46, 65664\n\t"
        "s_mov_b64 exec, s[52:53]\n\t"
        "buffer_load_dwordx4 v19, s[28:31], s6 offen offset:896 lds\n\t"
        "s_mov_b64 exec, -1\n\t"
        "v_exp_f32 v116, v116\n\t"
        "v_exp_f32 v117, v117\n\t"
        "v_exp_f32 v118, v118\n\t"
        "v_exp_f32 v119, v119\n\t"
        "v_smfmac_f32_32x32x32_bf16 v[74:89], v[162:165], v[106:113], v26\n\t"
        "ds_read_b64_tr_b16 v[162:163], v36 offset:36864\n\t"
        "ds_read_b64_tr_b16 v[164:165], v37 offset:36864\n\t"
        "v_exp_f32 v120, v120\n\t"
        "v_exp_f32 v121, v121\n\t"
        "v_exp_f32 v122, v122\n\t"
        "v_exp_f32 v123, v123\n\t"
        "v_smfmac_f32_32x32x32_bf16 v[58:73], v[170:173], v[98:105], v26\n\t"
        "s_waitcnt lgkmcnt(2)\n\t"
        "s_setprio 2\n\t"
        "v_exp_f32 v124, v124\n\t"
        "v_exp_f32 v125, v125\n\t"
        "v_exp_f32 v126, v126\n\t"
        "v_exp_f32 v127, v127\n\t"
        "v_smfmac_f32_32x32x32_bf16 v[74:89], v[170:173], v[114:121], v26\n\t"
        "ds_read_b64_tr_b16 v[170:171], v36 offset:37888\n\t"
        "ds_read_b64_tr_b16 v[172:173], v37 offset:37888\n\t"
        "v_exp_f32 v128, v128\n\t"
        "v_exp_f32 v129, v129\n\t"
        "v_exp_f32 v138, v138\n\t"
        "v_exp_f32 v139, v139\n\t"
        "v_mfma_f32_32x32x16_bf16 v[90:105], v[154:157], v[42:45], 0\n\t"
        "s_add_u32 m0, s46, 65792\n\t"
        "s_mov_b64 exec, s[52:53]\n\t"
        "buffer_load_dwordx4 v19, s[28:31], s6 offen offset:1792 lds\n\t"
        "s_mov_b64 exec, -1\n\t"
        "s_waitcnt vmcnt(3)\n\t"
        "v_exp_f32 v140, v140\n\t"
        "v_exp_f32 v141, v141\n\t"
        "v_exp_f32 v142, v142\n\t"
        "v_exp_f32 v143, v143\n\t"
        "v_mfma_f32_32x32x16_bf16 v[106:121], v[154:157], v[50:53], 0\n\t"
        "ds_read_b128 v[154:157], v15 offset:38912\n\t"
        "v_exp_f32 v144, v144\n\t"
        "v_exp_f32 v145, v145\n\t"
        "v_exp_f32 v130, v130\n\t"
        "v_exp_f32 v131, v131\n\t"
        "v_mfma_f32_32x32x16_bf16 v[90:105], v[158:161], v[46:49], v[90:105]\n\t"
        "ds_read_b64 v[178:179], v23\n\t"
        "v_exp_f32 v132, v132\n\t"
        "v_exp_f32 v133, v133\n\t"
        "v_exp_f32 v134, v134\n\t"
        "v_exp_f32 v135, v135\n\t"
        "v_mfma_f32_32x32x16_bf16 v[106:121], v[158:161], v[54:57], v[106:121]\n\t"
        "ds_read_b128 v[158:161], v15 offset:39936\n\t"
        "v_exp_f32 v136, v136\n\t"
        "v_exp_f32 v137, v137\n\t"
        "v_exp_f32 v146, v146\n\t"
        "v_exp_f32 v147, v147\n\t"
        "v_smfmac_f32_32x32x32_bf16 v[58:73], v[166:169], v[122:129], v26\n\t"
        "s_add_u32 m0, s46, 65920\n\t"
        "s_mov_b64 exec, s[52:53]\n\t"
        "buffer_load_dwordx4 v19, s[28:31], s6 offen offset:2688 lds\n\t"
        "s_mov_b64 exec, -1\n\t"
        "ds_read_b64 v[180:181], v23 offset:8\n\t"
        "ds_read_b64 v[182:183], v23 offset:16\n\t"
        "v_exp_f32 v148, v148\n\t"
        "v_exp_f32 v149, v149\n\t"
        "v_exp_f32 v150, v150\n\t"
        "v_exp_f32 v151, v151\n\t"
        "v_smfmac_f32_32x32x32_bf16 v[74:89], v[166:169], v[138:145], v26\n\t"
        "ds_read_b64_tr_b16 v[166:167], v36 offset:38912\n\t"
        "ds_read_b64_tr_b16 v[168:169], v37 offset:38912\n\t"
        "v_exp_f32 v152, v152\n\t"
        "v_exp_f32 v153, v153\n\t"
        "v_exp_f32 v90, v90\n\t"
        "v_exp_f32 v91, v91\n\t"
        "v_smfmac_f32_32x32x32_bf16 v[58:73], v[174:177], v[130:137], v26\n\t"
        "s_waitcnt lgkmcnt(4)\n\t"
        "v_exp_f32 v92, v92\n\t"
        "v_exp_f32 v93, v93\n\t"
        "v_exp_f32 v94, v94\n\t"
        "v_exp_f32 v95, v95\n\t"
        "v_smfmac_f32_32x32x32_bf16 v[74:89], v[174:177], v[146:153], v26\n\t"
        "ds_read_b64_tr_b16 v[174:175], v36 offset:39936\n\t"
        "ds_read_b64_tr_b16 v[176:177], v37 offset:39936\n\t"
        "v_exp_f32 v96, v96\n\t"
        "v_exp_f32 v97, v97\n\t"
        "v_exp_f32 v106, v106\n\t"
        "v_exp_f32 v107, v107\n\t"
        "v_mfma_f32_32x32x16_bf16 v[122:137], v[154:157], v[42:45], 0\n\t"
        "ds_read_b64 v[184:185], v23 offset:24\n\t"
        "v_exp_f32 v108, v108\n\t"
        "v_exp_f32 v109, v109\n\t"
        "v_exp_f32 v110, v110\n\t"
        "v_exp_f32 v111, v111\n\t"
        "v_mfma_f32_32x32x16_bf16 v[138:153], v[154:157], v[50:53], 0\n\t"
        "ds_read_b128 v[154:157], v15 offset:40960\n\t"
        "v_exp_f32 v112, v112\n\t"
        "v_exp_f32 v113, v113\n\t"
        "v_exp_f32 v98, v98\n\t"
        "v_exp_f32 v99, v99\n\t"
        "v_mfma_f32_32x32x16_bf16 v[122:137], v[158:161], v[46:49], v[122:137]\n\t"
        "ds_read_b64 v[186:187], v23 offset:32\n\t"
        "v_exp_f32 v100, v100\n\t"
        "v_exp_f32 v101, v101\n\t"
        "v_exp_f32 v102, v102\n\t"
        "v_exp_f32 v103, v103\n\t"
        "v_mfma_f32_32x32x16_bf16 v[138:153], v[158:161], v[54:57], v[138:153]\n\t"
        "ds_read_b128 v[158:161], v15 offset:41984\n\t"
        "v_exp_f32 v104, v104\n\t"
        "v_exp_f32 v105, v105\n\t"
        "v_exp_f32 v114, v114\n\t"
        "v_exp_f32 v115, v115\n\t"
        "v_smfmac_f32_32x32x32_bf16 v[58:73], v[162:165], v[90:97], v26\n\t"
        "ds_read_b64 v[188:189], v23 offset:40\n\t"
        "ds_read_b64 v[190:191], v23 offset:48\n\t"
        "v_exp_f32 v116, v116\n\t"
        "v_exp_f32 v117, v117\n\t"
        "v_exp_f32 v118, v118\n\t"
        "v_exp_f32 v119, v119\n\t"
        "v_smfmac_f32_32x32x32_bf16 v[74:89], v[162:165], v[106:113], v26\n\t"
        "ds_read_b64_tr_b16 v[162:163], v36 offset:40960\n\t"
        "ds_read_b64_tr_b16 v[164:165], v37 offset:40960\n\t"
        "v_exp_f32 v120, v120\n\t"
        "v_exp_f32 v121, v121\n\t"
        "v_exp_f32 v122, v122\n\t"
        "v_exp_f32 v123, v123\n\t"
        "v_smfmac_f32_32x32x32_bf16 v[58:73], v[170:173], v[98:105], v26\n\t"
        "s_waitcnt lgkmcnt(4)\n\t"
        "s_setprio 1\n\t"
        "v_exp_f32 v124, v124\n\t"
        "v_exp_f32 v125, v125\n\t"
        "v_exp_f32 v126, v126\n\t"
        "v_exp_f32 v127, v127\n\t"
        "v_smfmac_f32_32x32x32_bf16 v[74:89], v[170:173], v[114:121], v26\n\t"
        "ds_read_b64_tr_b16 v[170:171], v36 offset:41984\n\t"
        "ds_read_b64_tr_b16 v[172:173], v37 offset:41984\n\t"
        "v_exp_f32 v128, v128\n\t"
        "v_exp_f32 v129, v129\n\t"
        "v_exp_f32 v138, v138\n\t"
        "v_exp_f32 v139, v139\n\t"
        "v_mfma_f32_32x32x16_bf16 v[90:105], v[154:157], v[42:45], 0\n\t"
        "v_exp_f32 v140, v140\n\t"
        "v_exp_f32 v141, v141\n\t"
        "v_exp_f32 v142, v142\n\t"
        "v_exp_f32 v143, v143\n\t"
        "v_mfma_f32_32x32x16_bf16 v[106:121], v[154:157], v[50:53], 0\n\t"
        "s_waitcnt lgkmcnt(4)\n\t"
        "s_setprio 3\n\t"
        "v_dot2_f32_bf16 v208, v178, v178, 0\n\t"
        "v_dot2_f32_bf16 v209, v179, v179, 0\n\t"
        "v_dot2_f32_bf16 v208, v180, v180, v208\n\t"
        "v_dot2_f32_bf16 v209, v181, v181, v209\n\t"
        "v_dot2_f32_bf16 v208, v182, v182, v208\n\t"
        "v_dot2_f32_bf16 v209, v183, v183, v209\n\t"
        "v_dot2_f32_bf16 v208, v184, v184, v208\n\t"
        "v_dot2_f32_bf16 v209, v185, v185, v209\n\t"
        "v_dot2_f32_bf16 v208, v186, v186, v208\n\t"
        "v_dot2_f32_bf16 v209, v187, v187, v209\n\t"
        "v_dot2_f32_bf16 v208, v188, v188, v208\n\t"
        "v_dot2_f32_bf16 v209, v189, v189, v209\n\t"
        "v_dot2_f32_bf16 v208, v190, v190, v208\n\t"
        "v_dot2_f32_bf16 v209, v191, v191, v209\n\t"
        "s_nop 2\n\t"
        "v_add_f32 v208, v208, v209\n\t"
        "v_sqrt_f32 v208, v208\n\t"
        "s_nop 0\n\t"
        "v_add_f32 v208, 0x358637bd, v208\n\t"
        "v_rcp_f32 v208, v208\n\t"
        "s_nop 0\n\t"
        "v_cvt_pk_bf16_f32 v212, v208, 0\n\t"
        "v_cvt_pk_bf16_f32 v213, 0, v208\n\t"
        "v_dot2_f32_bf16 v214, v178, v212, 0\n\t"
        "v_dot2_f32_bf16 v215, v178, v213, 0\n\t"
        "v_dot2_f32_bf16 v216, v179, v212, 0\n\t"
        "v_dot2_f32_bf16 v217, v179, v213, 0\n\t"
        "v_dot2_f32_bf16 v218, v180, v212, 0\n\t"
        "v_dot2_f32_bf16 v219, v180, v213, 0\n\t"
        "v_cvt_pk_bf16_f32 v192, v214, v215\n\t"
        "v_dot2_f32_bf16 v220, v181, v212, 0\n\t"
        "v_dot2_f32_bf16 v221, v181, v213, 0\n\t"
        "v_cvt_pk_bf16_f32 v193, v216, v217\n\t"
        "v_dot2_f32_bf16 v214, v182, v212, 0\n\t"
        "v_dot2_f32_bf16 v215, v182, v213, 0\n\t"
        "v_cvt_pk_bf16_f32 v194, v218, v219\n\t"
        "v_dot2_f32_bf16 v216, v183, v212, 0\n\t"
        "v_dot2_f32_bf16 v217, v183, v213, 0\n\t"
        "v_cvt_pk_bf16_f32 v195, v220, v221\n\t"
        "v_dot2_f32_bf16 v218, v184, v212, 0\n\t"
        "v_dot2_f32_bf16 v219, v184, v213, 0\n\t"
        "v_cvt_pk_bf16_f32 v196, v214, v215\n\t"
        "v_dot2_f32_bf16 v220, v185, v212, 0\n\t"
        "v_dot2_f32_bf16 v221, v185, v213, 0\n\t"
        "v_cvt_pk_bf16_f32 v197, v216, v217\n\t"
        "v_dot2_f32_bf16 v214, v186, v212, 0\n\t"
        "v_dot2_f32_bf16 v215, v186, v213, 0\n\t"
        "v_cvt_pk_bf16_f32 v198, v218, v219\n\t"
        "v_dot2_f32_bf16 v216, v187, v212, 0\n\t"
        "v_dot2_f32_bf16 v217, v187, v213, 0\n\t"
        "v_cvt_pk_bf16_f32 v200, v220, v221\n\t"
        "v_dot2_f32_bf16 v218, v188, v212, 0\n\t"
        "v_dot2_f32_bf16 v219, v188, v213, 0\n\t"
        "v_cvt_pk_bf16_f32 v201, v214, v215\n\t"
        "v_dot2_f32_bf16 v220, v189, v212, 0\n\t"
        "v_dot2_f32_bf16 v221, v189, v213, 0\n\t"
        "v_cvt_pk_bf16_f32 v202, v216, v217\n\t"
        "v_dot2_f32_bf16 v214, v190, v212, 0\n\t"
        "v_dot2_f32_bf16 v215, v190, v213, 0\n\t"
        "v_cvt_pk_bf16_f32 v203, v218, v219\n\t"
        "v_dot2_f32_bf16 v216, v191, v212, 0\n\t"
        "v_dot2_f32_bf16 v217, v191, v213, 0\n\t"
        "v_cvt_pk_bf16_f32 v204, v220, v221\n\t"
        "v_cvt_pk_bf16_f32 v205, v214, v215\n\t"
        "s_nop 0\n\t"
        "v_cvt_pk_bf16_f32 v206, v216, v217\n\t"
        "s_setprio 1\n\t"
        "ds_read_b128 v[154:157], v15 offset:43008\n\t"
        "v_exp_f32 v144, v144\n\t"
        "v_exp_f32 v145, v145\n\t"
        "v_exp_f32 v130, v130\n\t"
        "v_exp_f32 v131, v131\n\t"
        "v_mfma_f32_32x32x16_bf16 v[90:105], v[158:161], v[46:49], v[90:105]\n\t"
        "ds_write_b128 v16, v[192:195]\n\t"
        "v_exp_f32 v132, v132\n\t"
        "v_exp_f32 v133, v133\n\t"
        "v_exp_f32 v134, v134\n\t"
        "v_exp_f32 v135, v135\n\t"
        "v_mfma_f32_32x32x16_bf16 v[106:121], v[158:161], v[54:57], v[106:121]\n\t"
        "ds_read_b128 v[158:161], v15 offset:44032\n\t"
        "v_exp_f32 v136, v136\n\t"
        "v_exp_f32 v137, v137\n\t"
        "v_exp_f32 v146, v146\n\t"
        "v_exp_f32 v147, v147\n\t"
        "v_smfmac_f32_32x32x32_bf16 v[58:73], v[166:169], v[122:129], v26\n\t"
        "ds_write_b128 v16, v[196:199] offset:1024\n\t"
        "v_exp_f32 v148, v148\n\t"
        "v_exp_f32 v149, v149\n\t"
        "v_exp_f32 v150, v150\n\t"
        "v_exp_f32 v151, v151\n\t"
        "v_smfmac_f32_32x32x32_bf16 v[74:89], v[166:169], v[138:145], v26\n\t"
        "ds_read_b64_tr_b16 v[166:167], v36 offset:43008\n\t"
        "ds_read_b64_tr_b16 v[168:169], v37 offset:43008\n\t"
        "v_exp_f32 v152, v152\n\t"
        "v_exp_f32 v153, v153\n\t"
        "v_exp_f32 v90, v90\n\t"
        "v_exp_f32 v91, v91\n\t"
        "v_smfmac_f32_32x32x32_bf16 v[58:73], v[174:177], v[130:137], v26\n\t"
        "s_waitcnt lgkmcnt(3)\n\t"
        "v_exp_f32 v92, v92\n\t"
        "v_exp_f32 v93, v93\n\t"
        "v_exp_f32 v94, v94\n\t"
        "v_exp_f32 v95, v95\n\t"
        "v_smfmac_f32_32x32x32_bf16 v[74:89], v[174:177], v[146:153], v26\n\t"
        "ds_read_b64_tr_b16 v[174:175], v36 offset:44032\n\t"
        "ds_read_b64_tr_b16 v[176:177], v37 offset:44032\n\t"
        "v_exp_f32 v96, v96\n\t"
        "v_exp_f32 v97, v97\n\t"
        "v_exp_f32 v106, v106\n\t"
        "v_exp_f32 v107, v107\n\t"
        "v_mfma_f32_32x32x16_bf16 v[122:137], v[154:157], v[42:45], 0\n\t"
        "ds_write_b128 v16, v[200:203] offset:512\n\t"
        "v_exp_f32 v108, v108\n\t"
        "v_exp_f32 v109, v109\n\t"
        "v_exp_f32 v110, v110\n\t"
        "v_exp_f32 v111, v111\n\t"
        "v_mfma_f32_32x32x16_bf16 v[138:153], v[154:157], v[50:53], 0\n\t"
        "ds_read_b128 v[154:157], v15 offset:45056\n\t"
        "v_exp_f32 v112, v112\n\t"
        "v_exp_f32 v113, v113\n\t"
        "v_exp_f32 v98, v98\n\t"
        "v_exp_f32 v99, v99\n\t"
        "v_mfma_f32_32x32x16_bf16 v[122:137], v[158:161], v[46:49], v[122:137]\n\t"
        "ds_write_b128 v16, v[204:207] offset:1536\n\t"
        "v_exp_f32 v100, v100\n\t"
        "v_exp_f32 v101, v101\n\t"
        "v_exp_f32 v102, v102\n\t"
        "v_exp_f32 v103, v103\n\t"
        "v_mfma_f32_32x32x16_bf16 v[138:153], v[158:161], v[54:57], v[138:153]\n\t"
        "ds_read_b128 v[158:161], v15 offset:46080\n\t"
        "v_exp_f32 v104, v104\n\t"
        "v_exp_f32 v105, v105\n\t"
        "v_exp_f32 v114, v114\n\t"
        "v_exp_f32 v115, v115\n\t"
        "v_smfmac_f32_32x32x32_bf16 v[58:73], v[162:165], v[90:97], v26\n\t"
        "s_mov_b32 m0, s48\n\t"
        "s_nop 0\n\t"
        "buffer_load_dwordx4 v14, s[20:23], s7 offen lds\n\t"
        "v_exp_f32 v116, v116\n\t"
        "v_exp_f32 v117, v117\n\t"
        "v_exp_f32 v118, v118\n\t"
        "v_exp_f32 v119, v119\n\t"
        "v_smfmac_f32_32x32x32_bf16 v[74:89], v[162:165], v[106:113], v26\n\t"
        "ds_read_b64_tr_b16 v[162:163], v36 offset:45056\n\t"
        "ds_read_b64_tr_b16 v[164:165], v37 offset:45056\n\t"
        "v_exp_f32 v120, v120\n\t"
        "v_exp_f32 v121, v121\n\t"
        "v_exp_f32 v122, v122\n\t"
        "v_exp_f32 v123, v123\n\t"
        "v_smfmac_f32_32x32x32_bf16 v[58:73], v[170:173], v[98:105], v26\n\t"
        "s_waitcnt lgkmcnt(2)\n\t"
        "s_setprio 0\n\t"
        "v_exp_f32 v124, v124\n\t"
        "v_exp_f32 v125, v125\n\t"
        "v_exp_f32 v126, v126\n\t"
        "v_exp_f32 v127, v127\n\t"
        "v_smfmac_f32_32x32x32_bf16 v[74:89], v[170:173], v[114:121], v26\n\t"
        "ds_read_b64_tr_b16 v[170:171], v36 offset:46080\n\t"
        "ds_read_b64_tr_b16 v[172:173], v37 offset:46080\n\t"
        "v_exp_f32 v128, v128\n\t"
        "v_exp_f32 v129, v129\n\t"
        "v_exp_f32 v138, v138\n\t"
        "v_exp_f32 v139, v139\n\t"
        "v_mfma_f32_32x32x16_bf16 v[90:105], v[154:157], v[42:45], 0\n\t"
        "buffer_load_dwordx4 v14, s[20:23], s7 offen offset:1024 lds\n\t"
        "v_exp_f32 v140, v140\n\t"
        "v_exp_f32 v141, v141\n\t"
        "v_exp_f32 v142, v142\n\t"
        "v_exp_f32 v143, v143\n\t"
        "v_mfma_f32_32x32x16_bf16 v[106:121], v[154:157], v[50:53], 0\n\t"
        "ds_read_b128 v[154:157], v15 offset:47104\n\t"
        "v_exp_f32 v144, v144\n\t"
        "v_exp_f32 v145, v145\n\t"
        "v_exp_f32 v130, v130\n\t"
        "v_exp_f32 v131, v131\n\t"
        "v_mfma_f32_32x32x16_bf16 v[90:105], v[158:161], v[46:49], v[90:105]\n\t"
        "buffer_load_dwordx4 v14, s[20:23], s7 offen offset:2048 lds\n\t"
        "v_exp_f32 v132, v132\n\t"
        "v_exp_f32 v133, v133\n\t"
        "v_exp_f32 v134, v134\n\t"
        "v_exp_f32 v135, v135\n\t"
        "v_mfma_f32_32x32x16_bf16 v[106:121], v[158:161], v[54:57], v[106:121]\n\t"
        "ds_read_b128 v[158:161], v15 offset:48128\n\t"
        "v_exp_f32 v136, v136\n\t"
        "v_exp_f32 v137, v137\n\t"
        "v_exp_f32 v146, v146\n\t"
        "v_exp_f32 v147, v147\n\t"
        "v_smfmac_f32_32x32x32_bf16 v[58:73], v[166:169], v[122:129], v26\n\t"
        "s_mov_b32 exec_hi, 0\n\t"
        "buffer_load_dwordx4 v14, s[20:23], s7 offen offset:3072 lds\n\t"
        "s_mov_b32 exec_hi, -1\n\t"
        "v_exp_f32 v148, v148\n\t"
        "v_exp_f32 v149, v149\n\t"
        "v_exp_f32 v150, v150\n\t"
        "v_exp_f32 v151, v151\n\t"
        "v_smfmac_f32_32x32x32_bf16 v[74:89], v[166:169], v[138:145], v26\n\t"
        "ds_read_b64_tr_b16 v[166:167], v36 offset:47104\n\t"
        "ds_read_b64_tr_b16 v[168:169], v37 offset:47104\n\t"
        "v_exp_f32 v152, v152\n\t"
        "v_exp_f32 v153, v153\n\t"
        "v_exp_f32 v90, v90\n\t"
        "v_exp_f32 v91, v91\n\t"
        "v_smfmac_f32_32x32x32_bf16 v[58:73], v[174:177], v[130:137], v26\n\t"
        "s_waitcnt lgkmcnt(2)\n\t"
        "v_exp_f32 v92, v92\n\t"
        "v_exp_f32 v93, v93\n\t"
        "v_exp_f32 v94, v94\n\t"
        "v_exp_f32 v95, v95\n\t"
        "v_smfmac_f32_32x32x32_bf16 v[74:89], v[174:177], v[146:153], v26\n\t"
        "ds_read_b64_tr_b16 v[174:175], v36 offset:48128\n\t"
        "ds_read_b64_tr_b16 v[176:177], v37 offset:48128\n\t"
        "v_exp_f32 v96, v96\n\t"
        "v_exp_f32 v97, v97\n\t"
        "v_exp_f32 v106, v106\n\t"
        "v_exp_f32 v107, v107\n\t"
        "v_mfma_f32_32x32x16_bf16 v[122:137], v[154:157], v[42:45], 0\n\t"
        "v_exp_f32 v108, v108\n\t"
        "v_exp_f32 v109, v109\n\t"
        "v_exp_f32 v110, v110\n\t"
        "v_exp_f32 v111, v111\n\t"
        "v_mfma_f32_32x32x16_bf16 v[138:153], v[154:157], v[50:53], 0\n\t"
        "v_exp_f32 v112, v112\n\t"
        "v_exp_f32 v113, v113\n\t"
        "v_exp_f32 v98, v98\n\t"
        "v_exp_f32 v99, v99\n\t"
        "v_mfma_f32_32x32x16_bf16 v[122:137], v[158:161], v[46:49], v[122:137]\n\t"
        "s_waitcnt vmcnt(4)\n\t"
        "v_exp_f32 v100, v100\n\t"
        "v_exp_f32 v101, v101\n\t"
        "v_exp_f32 v102, v102\n\t"
        "v_exp_f32 v103, v103\n\t"
        "v_mfma_f32_32x32x16_bf16 v[138:153], v[158:161], v[54:57], v[138:153]\n\t"
        "s_waitcnt lgkmcnt(0)\n\t"
        "s_barrier\n\t"
        "ds_read_b128 v[154:157], v15\n\t"
        "ds_read_b128 v[158:161], v15 offset:1024\n\t"
        "v_exp_f32 v104, v104\n\t"
        "v_exp_f32 v105, v105\n\t"
        "v_exp_f32 v114, v114\n\t"
        "v_exp_f32 v115, v115\n\t"
        "v_smfmac_f32_32x32x32_bf16 v[58:73], v[162:165], v[90:97], v26\n\t"
        "v_exp_f32 v116, v116\n\t"
        "v_exp_f32 v117, v117\n\t"
        "v_exp_f32 v118, v118\n\t"
        "v_exp_f32 v119, v119\n\t"
        "v_smfmac_f32_32x32x32_bf16 v[74:89], v[162:165], v[106:113], v26\n\t"
        "ds_read_b64_tr_b16 v[162:163], v36\n\t"
        "ds_read_b64_tr_b16 v[164:165], v37\n\t"
        "v_exp_f32 v120, v120\n\t"
        "v_exp_f32 v121, v121\n\t"
        "v_exp_f32 v122, v122\n\t"
        "v_exp_f32 v123, v123\n\t"
        "v_smfmac_f32_32x32x32_bf16 v[58:73], v[170:173], v[98:105], v26\n\t"
        "s_waitcnt lgkmcnt(2)\n\t"
        "s_setprio 3\n\t"
        "v_exp_f32 v124, v124\n\t"
        "v_exp_f32 v125, v125\n\t"
        "v_exp_f32 v126, v126\n\t"
        "v_exp_f32 v127, v127\n\t"
        "v_smfmac_f32_32x32x32_bf16 v[74:89], v[170:173], v[114:121], v26\n\t"
        "ds_read_b64_tr_b16 v[170:171], v36 offset:1024\n\t"
        "ds_read_b64_tr_b16 v[172:173], v37 offset:1024\n\t"
        "v_exp_f32 v128, v128\n\t"
        "v_exp_f32 v129, v129\n\t"
        "v_exp_f32 v138, v138\n\t"
        "v_exp_f32 v139, v139\n\t"
        "v_mfma_f32_32x32x16_bf16 v[90:105], v[154:157], v[42:45], 0\n\t"
        "s_add_u32 m0, s46, 98304\n\t"
        "s_mov_b64 exec, s[52:53]\n\t"
        "buffer_load_dwordx4 v19, s[28:31], s7 offen lds\n\t"
        "s_mov_b64 exec, -1\n\t"
        "v_exp_f32 v140, v140\n\t"
        "v_exp_f32 v141, v141\n\t"
        "v_exp_f32 v142, v142\n\t"
        "v_exp_f32 v143, v143\n\t"
        "v_mfma_f32_32x32x16_bf16 v[106:121], v[154:157], v[50:53], 0\n\t"
        "ds_read_b128 v[154:157], v15 offset:2048\n\t"
        "v_exp_f32 v144, v144\n\t"
        "v_exp_f32 v145, v145\n\t"
        "v_exp_f32 v130, v130\n\t"
        "v_exp_f32 v131, v131\n\t"
        "v_mfma_f32_32x32x16_bf16 v[90:105], v[158:161], v[46:49], v[90:105]\n\t"
        "v_exp_f32 v132, v132\n\t"
        "v_exp_f32 v133, v133\n\t"
        "v_exp_f32 v134, v134\n\t"
        "v_exp_f32 v135, v135\n\t"
        "v_mfma_f32_32x32x16_bf16 v[106:121], v[158:161], v[54:57], v[106:121]\n\t"
        "ds_read_b128 v[158:161], v15 offset:3072\n\t"
        "v_exp_f32 v136, v136\n\t"
        "v_exp_f32 v137, v137\n\t"
        "v_exp_f32 v146, v146\n\t"
        "v_exp_f32 v147, v147\n\t"
        "v_smfmac_f32_32x32x32_bf16 v[58:73], v[166:169], v[122:129], v26\n\t"
        "s_add_u32 m0, s46, 98432\n\t"
        "s_mov_b64 exec, s[52:53]\n\t"
        "buffer_load_dwordx4 v19, s[28:31], s7 offen offset:896 lds\n\t"
        "s_mov_b64 exec, -1\n\t"
        "v_exp_f32 v148, v148\n\t"
        "v_exp_f32 v149, v149\n\t"
        "v_exp_f32 v150, v150\n\t"
        "v_exp_f32 v151, v151\n\t"
        "v_smfmac_f32_32x32x32_bf16 v[74:89], v[166:169], v[138:145], v26\n\t"
        "ds_read_b64_tr_b16 v[166:167], v36 offset:2048\n\t"
        "ds_read_b64_tr_b16 v[168:169], v37 offset:2048\n\t"
        "v_exp_f32 v152, v152\n\t"
        "v_exp_f32 v153, v153\n\t"
        "v_exp_f32 v90, v90\n\t"
        "v_exp_f32 v91, v91\n\t"
        "v_smfmac_f32_32x32x32_bf16 v[58:73], v[174:177], v[130:137], v26\n\t"
        "s_waitcnt lgkmcnt(2)\n\t"
        "v_exp_f32 v92, v92\n\t"
        "v_exp_f32 v93, v93\n\t"
        "v_exp_f32 v94, v94\n\t"
        "v_exp_f32 v95, v95\n\t"
        "v_smfmac_f32_32x32x32_bf16 v[74:89], v[174:177], v[146:153], v26\n\t"
        "ds_read_b64_tr_b16 v[174:175], v36 offset:3072\n\t"
        "ds_read_b64_tr_b16 v[176:177], v37 offset:3072\n\t"
        "v_exp_f32 v96, v96\n\t"
        "v_exp_f32 v97, v97\n\t"
        "v_exp_f32 v106, v106\n\t"
        "v_exp_f32 v107, v107\n\t"
        "v_mfma_f32_32x32x16_bf16 v[122:137], v[154:157], v[42:45], 0\n\t"
        "s_add_u32 m0, s46, 98560\n\t"
        "s_mov_b64 exec, s[52:53]\n\t"
        "buffer_load_dwordx4 v19, s[28:31], s7 offen offset:1792 lds\n\t"
        "s_mov_b64 exec, -1\n\t"
        "v_exp_f32 v108, v108\n\t"
        "v_exp_f32 v109, v109\n\t"
        "v_exp_f32 v110, v110\n\t"
        "v_exp_f32 v111, v111\n\t"
        "v_mfma_f32_32x32x16_bf16 v[138:153], v[154:157], v[50:53], 0\n\t"
        "ds_read_b128 v[154:157], v15 offset:4096\n\t"
        "v_exp_f32 v112, v112\n\t"
        "v_exp_f32 v113, v113\n\t"
        "v_exp_f32 v98, v98\n\t"
        "v_exp_f32 v99, v99\n\t"
        "v_mfma_f32_32x32x16_bf16 v[122:137], v[158:161], v[46:49], v[122:137]\n\t"
        "v_exp_f32 v100, v100\n\t"
        "v_exp_f32 v101, v101\n\t"
        "v_exp_f32 v102, v102\n\t"
        "v_exp_f32 v103, v103\n\t"
        "v_mfma_f32_32x32x16_bf16 v[138:153], v[158:161], v[54:57], v[138:153]\n\t"
        "ds_read_b128 v[158:161], v15 offset:5120\n\t"
        "v_exp_f32 v104, v104\n\t"
        "v_exp_f32 v105, v105\n\t"
        "v_exp_f32 v114, v114\n\t"
        "v_exp_f32 v115, v115\n\t"
        "v_smfmac_f32_32x32x32_bf16 v[58:73], v[162:165], v[90:97], v26\n\t"
        "s_add_u32 m0, s46, 98688\n\t"
        "s_mov_b64 exec, s[52:53]\n\t"
        "buffer_load_dwordx4 v19, s[28:31], s7 offen offset:2688 lds\n\t"
        "s_mov_b64 exec, -1\n\t"
        "v_exp_f32 v116, v116\n\t"
        "v_exp_f32 v117, v117\n\t"
        "v_exp_f32 v118, v118\n\t"
        "v_exp_f32 v119, v119\n\t"
        "v_smfmac_f32_32x32x32_bf16 v[74:89], v[162:165], v[106:113], v26\n\t"
        "ds_read_b64_tr_b16 v[162:163], v36 offset:4096\n\t"
        "ds_read_b64_tr_b16 v[164:165], v37 offset:4096\n\t"
        "v_exp_f32 v120, v120\n\t"
        "v_exp_f32 v121, v121\n\t"
        "v_exp_f32 v122, v122\n\t"
        "v_exp_f32 v123, v123\n\t"
        "v_smfmac_f32_32x32x32_bf16 v[58:73], v[170:173], v[98:105], v26\n\t"
        "s_waitcnt lgkmcnt(2)\n\t"
        "s_setprio 2\n\t"
        "v_exp_f32 v124, v124\n\t"
        "v_exp_f32 v125, v125\n\t"
        "v_exp_f32 v126, v126\n\t"
        "v_exp_f32 v127, v127\n\t"
        "v_smfmac_f32_32x32x32_bf16 v[74:89], v[170:173], v[114:121], v26\n\t"
        "ds_read_b64_tr_b16 v[170:171], v36 offset:5120\n\t"
        "ds_read_b64_tr_b16 v[172:173], v37 offset:5120\n\t"
        "v_exp_f32 v128, v128\n\t"
        "v_exp_f32 v129, v129\n\t"
        "v_exp_f32 v138, v138\n\t"
        "v_exp_f32 v139, v139\n\t"
        "v_mfma_f32_32x32x16_bf16 v[90:105], v[154:157], v[42:45], 0\n\t"
        "s_waitcnt vmcnt(4)\n\t"
        "v_exp_f32 v140, v140\n\t"
        "v_exp_f32 v141, v141\n\t"
        "v_exp_f32 v142, v142\n\t"
        "v_exp_f32 v143, v143\n\t"
        "v_mfma_f32_32x32x16_bf16 v[106:121], v[154:157], v[50:53], 0\n\t"
        "ds_read_b128 v[154:157], v15 offset:6144\n\t"
        "v_exp_f32 v144, v144\n\t"
        "v_exp_f32 v145, v145\n\t"
        "v_exp_f32 v130, v130\n\t"
        "v_exp_f32 v131, v131\n\t"
        "v_mfma_f32_32x32x16_bf16 v[90:105], v[158:161], v[46:49], v[90:105]\n\t"
        "ds_read_b64 v[178:179], v23\n\t"
        "v_exp_f32 v132, v132\n\t"
        "v_exp_f32 v133, v133\n\t"
        "v_exp_f32 v134, v134\n\t"
        "v_exp_f32 v135, v135\n\t"
        "v_mfma_f32_32x32x16_bf16 v[106:121], v[158:161], v[54:57], v[106:121]\n\t"
        "ds_read_b128 v[158:161], v15 offset:7168\n\t"
        "v_exp_f32 v136, v136\n\t"
        "v_exp_f32 v137, v137\n\t"
        "v_exp_f32 v146, v146\n\t"
        "v_exp_f32 v147, v147\n\t"
        "v_smfmac_f32_32x32x32_bf16 v[58:73], v[166:169], v[122:129], v26\n\t"
        "ds_read_b64 v[180:181], v23 offset:8\n\t"
        "ds_read_b64 v[182:183], v23 offset:16\n\t"
        "v_exp_f32 v148, v148\n\t"
        "v_exp_f32 v149, v149\n\t"
        "v_exp_f32 v150, v150\n\t"
        "v_exp_f32 v151, v151\n\t"
        "v_smfmac_f32_32x32x32_bf16 v[74:89], v[166:169], v[138:145], v26\n\t"
        "ds_read_b64_tr_b16 v[166:167], v36 offset:6144\n\t"
        "ds_read_b64_tr_b16 v[168:169], v37 offset:6144\n\t"
        "v_exp_f32 v152, v152\n\t"
        "v_exp_f32 v153, v153\n\t"
        "v_exp_f32 v90, v90\n\t"
        "v_exp_f32 v91, v91\n\t"
        "v_smfmac_f32_32x32x32_bf16 v[58:73], v[174:177], v[130:137], v26\n\t"
        "s_waitcnt lgkmcnt(4)\n\t"
        "v_exp_f32 v92, v92\n\t"
        "v_exp_f32 v93, v93\n\t"
        "v_exp_f32 v94, v94\n\t"
        "v_exp_f32 v95, v95\n\t"
        "v_smfmac_f32_32x32x32_bf16 v[74:89], v[174:177], v[146:153], v26\n\t"
        "ds_read_b64_tr_b16 v[174:175], v36 offset:7168\n\t"
        "ds_read_b64_tr_b16 v[176:177], v37 offset:7168\n\t"
        "v_exp_f32 v96, v96\n\t"
        "v_exp_f32 v97, v97\n\t"
        "v_exp_f32 v106, v106\n\t"
        "v_exp_f32 v107, v107\n\t"
        "v_mfma_f32_32x32x16_bf16 v[122:137], v[154:157], v[42:45], 0\n\t"
        "ds_read_b64 v[184:185], v23 offset:24\n\t"
        "v_exp_f32 v108, v108\n\t"
        "v_exp_f32 v109, v109\n\t"
        "v_exp_f32 v110, v110\n\t"
        "v_exp_f32 v111, v111\n\t"
        "v_mfma_f32_32x32x16_bf16 v[138:153], v[154:157], v[50:53], 0\n\t"
        "ds_read_b128 v[154:157], v15 offset:8192\n\t"
        "v_exp_f32 v112, v112\n\t"
        "v_exp_f32 v113, v113\n\t"
        "v_exp_f32 v98, v98\n\t"
        "v_exp_f32 v99, v99\n\t"
        "v_mfma_f32_32x32x16_bf16 v[122:137], v[158:161], v[46:49], v[122:137]\n\t"
        "ds_read_b64 v[186:187], v23 offset:32\n\t"
        "v_exp_f32 v100, v100\n\t"
        "v_exp_f32 v101, v101\n\t"
        "v_exp_f32 v102, v102\n\t"
        "v_exp_f32 v103, v103\n\t"
        "v_mfma_f32_32x32x16_bf16 v[138:153], v[158:161], v[54:57], v[138:153]\n\t"
        "ds_read_b128 v[158:161], v15 offset:9216\n\t"
        "v_exp_f32 v104, v104\n\t"
        "v_exp_f32 v105, v105\n\t"
        "v_exp_f32 v114, v114\n\t"
        "v_exp_f32 v115, v115\n\t"
        "v_smfmac_f32_32x32x32_bf16 v[58:73], v[162:165], v[90:97], v26\n\t"
        "ds_read_b64 v[188:189], v23 offset:40\n\t"
        "ds_read_b64 v[190:191], v23 offset:48\n\t"
        "v_exp_f32 v116, v116\n\t"
        "v_exp_f32 v117, v117\n\t"
        "v_exp_f32 v118, v118\n\t"
        "v_exp_f32 v119, v119\n\t"
        "v_smfmac_f32_32x32x32_bf16 v[74:89], v[162:165], v[106:113], v26\n\t"
        "ds_read_b64_tr_b16 v[162:163], v36 offset:8192\n\t"
        "ds_read_b64_tr_b16 v[164:165], v37 offset:8192\n\t"
        "v_exp_f32 v120, v120\n\t"
        "v_exp_f32 v121, v121\n\t"
        "v_exp_f32 v122, v122\n\t"
        "v_exp_f32 v123, v123\n\t"
        "v_smfmac_f32_32x32x32_bf16 v[58:73], v[170:173], v[98:105], v26\n\t"
        "s_waitcnt lgkmcnt(4)\n\t"
        "s_setprio 1\n\t"
        "v_exp_f32 v124, v124\n\t"
        "v_exp_f32 v125, v125\n\t"
        "v_exp_f32 v126, v126\n\t"
        "v_exp_f32 v127, v127\n\t"
        "v_smfmac_f32_32x32x32_bf16 v[74:89], v[170:173], v[114:121], v26\n\t"
        "ds_read_b64_tr_b16 v[170:171], v36 offset:9216\n\t"
        "ds_read_b64_tr_b16 v[172:173], v37 offset:9216\n\t"
        "v_exp_f32 v128, v128\n\t"
        "v_exp_f32 v129, v129\n\t"
        "v_exp_f32 v138, v138\n\t"
        "v_exp_f32 v139, v139\n\t"
        "v_mfma_f32_32x32x16_bf16 v[90:105], v[154:157], v[42:45], 0\n\t"
        "v_exp_f32 v140, v140\n\t"
        "v_exp_f32 v141, v141\n\t"
        "v_exp_f32 v142, v142\n\t"
        "v_exp_f32 v143, v143\n\t"
        "v_mfma_f32_32x32x16_bf16 v[106:121], v[154:157], v[50:53], 0\n\t"
        "s_waitcnt lgkmcnt(4)\n\t"
        "s_setprio 3\n\t"
        "v_dot2_f32_bf16 v208, v178, v178, 0\n\t"
        "v_dot2_f32_bf16 v209, v179, v179, 0\n\t"
        "v_dot2_f32_bf16 v208, v180, v180, v208\n\t"
        "v_dot2_f32_bf16 v209, v181, v181, v209\n\t"
        "v_dot2_f32_bf16 v208, v182, v182, v208\n\t"
        "v_dot2_f32_bf16 v209, v183, v183, v209\n\t"
        "v_dot2_f32_bf16 v208, v184, v184, v208\n\t"
        "v_dot2_f32_bf16 v209, v185, v185, v209\n\t"
        "v_dot2_f32_bf16 v208, v186, v186, v208\n\t"
        "v_dot2_f32_bf16 v209, v187, v187, v209\n\t"
        "v_dot2_f32_bf16 v208, v188, v188, v208\n\t"
        "v_dot2_f32_bf16 v209, v189, v189, v209\n\t"
        "v_dot2_f32_bf16 v208, v190, v190, v208\n\t"
        "v_dot2_f32_bf16 v209, v191, v191, v209\n\t"
        "s_nop 2\n\t"
        "v_add_f32 v208, v208, v209\n\t"
        "v_sqrt_f32 v208, v208\n\t"
        "s_nop 0\n\t"
        "v_add_f32 v208, 0x358637bd, v208\n\t"
        "v_rcp_f32 v208, v208\n\t"
        "s_nop 0\n\t"
        "v_cvt_pk_bf16_f32 v212, v208, 0\n\t"
        "v_cvt_pk_bf16_f32 v213, 0, v208\n\t"
        "v_dot2_f32_bf16 v214, v178, v212, 0\n\t"
        "v_dot2_f32_bf16 v215, v178, v213, 0\n\t"
        "v_dot2_f32_bf16 v216, v179, v212, 0\n\t"
        "v_dot2_f32_bf16 v217, v179, v213, 0\n\t"
        "v_dot2_f32_bf16 v218, v180, v212, 0\n\t"
        "v_dot2_f32_bf16 v219, v180, v213, 0\n\t"
        "v_cvt_pk_bf16_f32 v192, v214, v215\n\t"
        "v_dot2_f32_bf16 v220, v181, v212, 0\n\t"
        "v_dot2_f32_bf16 v221, v181, v213, 0\n\t"
        "v_cvt_pk_bf16_f32 v193, v216, v217\n\t"
        "v_dot2_f32_bf16 v214, v182, v212, 0\n\t"
        "v_dot2_f32_bf16 v215, v182, v213, 0\n\t"
        "v_cvt_pk_bf16_f32 v194, v218, v219\n\t"
        "v_dot2_f32_bf16 v216, v183, v212, 0\n\t"
        "v_dot2_f32_bf16 v217, v183, v213, 0\n\t"
        "v_cvt_pk_bf16_f32 v195, v220, v221\n\t"
        "v_dot2_f32_bf16 v218, v184, v212, 0\n\t"
        "v_dot2_f32_bf16 v219, v184, v213, 0\n\t"
        "v_cvt_pk_bf16_f32 v196, v214, v215\n\t"
        "v_dot2_f32_bf16 v220, v185, v212, 0\n\t"
        "v_dot2_f32_bf16 v221, v185, v213, 0\n\t"
        "v_cvt_pk_bf16_f32 v197, v216, v217\n\t"
        "v_dot2_f32_bf16 v214, v186, v212, 0\n\t"
        "v_dot2_f32_bf16 v215, v186, v213, 0\n\t"
        "v_cvt_pk_bf16_f32 v198, v218, v219\n\t"
        "v_dot2_f32_bf16 v216, v187, v212, 0\n\t"
        "v_dot2_f32_bf16 v217, v187, v213, 0\n\t"
        "v_cvt_pk_bf16_f32 v200, v220, v221\n\t"
        "v_dot2_f32_bf16 v218, v188, v212, 0\n\t"
        "v_dot2_f32_bf16 v219, v188, v213, 0\n\t"
        "v_cvt_pk_bf16_f32 v201, v214, v215\n\t"
        "v_dot2_f32_bf16 v220, v189, v212, 0\n\t"
        "v_dot2_f32_bf16 v221, v189, v213, 0\n\t"
        "v_cvt_pk_bf16_f32 v202, v216, v217\n\t"
        "v_dot2_f32_bf16 v214, v190, v212, 0\n\t"
        "v_dot2_f32_bf16 v215, v190, v213, 0\n\t"
        "v_cvt_pk_bf16_f32 v203, v218, v219\n\t"
        "v_dot2_f32_bf16 v216, v191, v212, 0\n\t"
        "v_dot2_f32_bf16 v217, v191, v213, 0\n\t"
        "v_cvt_pk_bf16_f32 v204, v220, v221\n\t"
        "v_cvt_pk_bf16_f32 v205, v214, v215\n\t"
        "s_nop 0\n\t"
        "v_cvt_pk_bf16_f32 v206, v216, v217\n\t"
        "s_setprio 1\n\t"
        "ds_read_b128 v[154:157], v15 offset:10240\n\t"
        "v_exp_f32 v144, v144\n\t"
        "v_exp_f32 v145, v145\n\t"
        "v_exp_f32 v130, v130\n\t"
        "v_exp_f32 v131, v131\n\t"
        "v_mfma_f32_32x32x16_bf16 v[90:105], v[158:161], v[46:49], v[90:105]\n\t"
        "ds_write_b128 v16, v[192:195] offset:32768\n\t"
        "v_exp_f32 v132, v132\n\t"
        "v_exp_f32 v133, v133\n\t"
        "v_exp_f32 v134, v134\n\t"
        "v_exp_f32 v135, v135\n\t"
        "v_mfma_f32_32x32x16_bf16 v[106:121], v[158:161], v[54:57], v[106:121]\n\t"
        "ds_read_b128 v[158:161], v15 offset:11264\n\t"
        "v_exp_f32 v136, v136\n\t"
        "v_exp_f32 v137, v137\n\t"
        "v_exp_f32 v146, v146\n\t"
        "v_exp_f32 v147, v147\n\t"
        "v_smfmac_f32_32x32x32_bf16 v[58:73], v[166:169], v[122:129], v26\n\t"
        "ds_write_b128 v16, v[196:199] offset:33792\n\t"
        "v_exp_f32 v148, v148\n\t"
        "v_exp_f32 v149, v149\n\t"
        "v_exp_f32 v150, v150\n\t"
        "v_exp_f32 v151, v151\n\t"
        "v_smfmac_f32_32x32x32_bf16 v[74:89], v[166:169], v[138:145], v26\n\t"
        "ds_read_b64_tr_b16 v[166:167], v36 offset:10240\n\t"
        "ds_read_b64_tr_b16 v[168:169], v37 offset:10240\n\t"
        "v_exp_f32 v152, v152\n\t"
        "v_exp_f32 v153, v153\n\t"
        "v_exp_f32 v90, v90\n\t"
        "v_exp_f32 v91, v91\n\t"
        "v_smfmac_f32_32x32x32_bf16 v[58:73], v[174:177], v[130:137], v26\n\t"
        "s_waitcnt lgkmcnt(3)\n\t"
        "v_exp_f32 v92, v92\n\t"
        "v_exp_f32 v93, v93\n\t"
        "v_exp_f32 v94, v94\n\t"
        "v_exp_f32 v95, v95\n\t"
        "v_smfmac_f32_32x32x32_bf16 v[74:89], v[174:177], v[146:153], v26\n\t"
        "ds_read_b64_tr_b16 v[174:175], v36 offset:11264\n\t"
        "ds_read_b64_tr_b16 v[176:177], v37 offset:11264\n\t"
        "v_exp_f32 v96, v96\n\t"
        "v_exp_f32 v97, v97\n\t"
        "v_exp_f32 v106, v106\n\t"
        "v_exp_f32 v107, v107\n\t"
        "v_mfma_f32_32x32x16_bf16 v[122:137], v[154:157], v[42:45], 0\n\t"
        "ds_write_b128 v16, v[200:203] offset:33280\n\t"
        "v_exp_f32 v108, v108\n\t"
        "v_exp_f32 v109, v109\n\t"
        "v_exp_f32 v110, v110\n\t"
        "v_exp_f32 v111, v111\n\t"
        "v_mfma_f32_32x32x16_bf16 v[138:153], v[154:157], v[50:53], 0\n\t"
        "ds_read_b128 v[154:157], v15 offset:12288\n\t"
        "v_exp_f32 v112, v112\n\t"
        "v_exp_f32 v113, v113\n\t"
        "v_exp_f32 v98, v98\n\t"
        "v_exp_f32 v99, v99\n\t"
        "v_mfma_f32_32x32x16_bf16 v[122:137], v[158:161], v[46:49], v[122:137]\n\t"
        "ds_write_b128 v16, v[204:207] offset:34304\n\t"
        "v_exp_f32 v100, v100\n\t"
        "v_exp_f32 v101, v101\n\t"
        "v_exp_f32 v102, v102\n\t"
        "v_exp_f32 v103, v103\n\t"
        "v_mfma_f32_32x32x16_bf16 v[138:153], v[158:161], v[54:57], v[138:153]\n\t"
        "ds_read_b128 v[158:161], v15 offset:13312\n\t"
        "v_exp_f32 v104, v104\n\t"
        "v_exp_f32 v105, v105\n\t"
        "v_exp_f32 v114, v114\n\t"
        "v_exp_f32 v115, v115\n\t"
        "v_smfmac_f32_32x32x32_bf16 v[58:73], v[162:165], v[90:97], v26\n\t"
        "s_mov_b32 m0, s48\n\t"
        "s_nop 0\n\t"
        "buffer_load_dwordx4 v14, s[20:23], s18 offen lds\n\t"
        "v_exp_f32 v116, v116\n\t"
        "v_exp_f32 v117, v117\n\t"
        "v_exp_f32 v118, v118\n\t"
        "v_exp_f32 v119, v119\n\t"
        "v_smfmac_f32_32x32x32_bf16 v[74:89], v[162:165], v[106:113], v26\n\t"
        "ds_read_b64_tr_b16 v[162:163], v36 offset:12288\n\t"
        "ds_read_b64_tr_b16 v[164:165], v37 offset:12288\n\t"
        "v_exp_f32 v120, v120\n\t"
        "v_exp_f32 v121, v121\n\t"
        "v_exp_f32 v122, v122\n\t"
        "v_exp_f32 v123, v123\n\t"
        "v_smfmac_f32_32x32x32_bf16 v[58:73], v[170:173], v[98:105], v26\n\t"
        "s_waitcnt lgkmcnt(2)\n\t"
        "s_setprio 0\n\t"
        "v_exp_f32 v124, v124\n\t"
        "v_exp_f32 v125, v125\n\t"
        "v_exp_f32 v126, v126\n\t"
        "v_exp_f32 v127, v127\n\t"
        "v_smfmac_f32_32x32x32_bf16 v[74:89], v[170:173], v[114:121], v26\n\t"
        "ds_read_b64_tr_b16 v[170:171], v36 offset:13312\n\t"
        "ds_read_b64_tr_b16 v[172:173], v37 offset:13312\n\t"
        "v_exp_f32 v128, v128\n\t"
        "v_exp_f32 v129, v129\n\t"
        "v_exp_f32 v138, v138\n\t"
        "v_exp_f32 v139, v139\n\t"
        "v_mfma_f32_32x32x16_bf16 v[90:105], v[154:157], v[42:45], 0\n\t"
        "buffer_load_dwordx4 v14, s[20:23], s18 offen offset:1024 lds\n\t"
        "v_exp_f32 v140, v140\n\t"
        "v_exp_f32 v141, v141\n\t"
        "v_exp_f32 v142, v142\n\t"
        "v_exp_f32 v143, v143\n\t"
        "v_mfma_f32_32x32x16_bf16 v[106:121], v[154:157], v[50:53], 0\n\t"
        "ds_read_b128 v[154:157], v15 offset:14336\n\t"
        "v_exp_f32 v144, v144\n\t"
        "v_exp_f32 v145, v145\n\t"
        "v_exp_f32 v130, v130\n\t"
        "v_exp_f32 v131, v131\n\t"
        "v_mfma_f32_32x32x16_bf16 v[90:105], v[158:161], v[46:49], v[90:105]\n\t"
        "buffer_load_dwordx4 v14, s[20:23], s18 offen offset:2048 lds\n\t"
        "v_exp_f32 v132, v132\n\t"
        "v_exp_f32 v133, v133\n\t"
        "v_exp_f32 v134, v134\n\t"
        "v_exp_f32 v135, v135\n\t"
        "v_mfma_f32_32x32x16_bf16 v[106:121], v[158:161], v[54:57], v[106:121]\n\t"
        "ds_read_b128 v[158:161], v15 offset:15360\n\t"
        "v_exp_f32 v136, v136\n\t"
        "v_exp_f32 v137, v137\n\t"
        "v_exp_f32 v146, v146\n\t"
        "v_exp_f32 v147, v147\n\t"
        "v_smfmac_f32_32x32x32_bf16 v[58:73], v[166:169], v[122:129], v26\n\t"
        "s_mov_b32 exec_hi, 0\n\t"
        "buffer_load_dwordx4 v14, s[20:23], s18 offen offset:3072 lds\n\t"
        "s_mov_b32 exec_hi, -1\n\t"
        "v_exp_f32 v148, v148\n\t"
        "v_exp_f32 v149, v149\n\t"
        "v_exp_f32 v150, v150\n\t"
        "v_exp_f32 v151, v151\n\t"
        "v_smfmac_f32_32x32x32_bf16 v[74:89], v[166:169], v[138:145], v26\n\t"
        "ds_read_b64_tr_b16 v[166:167], v36 offset:14336\n\t"
        "ds_read_b64_tr_b16 v[168:169], v37 offset:14336\n\t"
        "v_exp_f32 v152, v152\n\t"
        "v_exp_f32 v153, v153\n\t"
        "v_exp_f32 v90, v90\n\t"
        "v_exp_f32 v91, v91\n\t"
        "v_smfmac_f32_32x32x32_bf16 v[58:73], v[174:177], v[130:137], v26\n\t"
        "s_waitcnt lgkmcnt(2)\n\t"
        "v_exp_f32 v92, v92\n\t"
        "v_exp_f32 v93, v93\n\t"
        "v_exp_f32 v94, v94\n\t"
        "v_exp_f32 v95, v95\n\t"
        "v_smfmac_f32_32x32x32_bf16 v[74:89], v[174:177], v[146:153], v26\n\t"
        "ds_read_b64_tr_b16 v[174:175], v36 offset:15360\n\t"
        "ds_read_b64_tr_b16 v[176:177], v37 offset:15360\n\t"
        "v_exp_f32 v96, v96\n\t"
        "v_exp_f32 v97, v97\n\t"
        "v_exp_f32 v106, v106\n\t"
        "v_exp_f32 v107, v107\n\t"
        "v_mfma_f32_32x32x16_bf16 v[122:137], v[154:157], v[42:45], 0\n\t"
        "v_exp_f32 v108, v108\n\t"
        "v_exp_f32 v109, v109\n\t"
        "v_exp_f32 v110, v110\n\t"
        "v_exp_f32 v111, v111\n\t"
        "v_mfma_f32_32x32x16_bf16 v[138:153], v[154:157], v[50:53], 0\n\t"
        "v_exp_f32 v112, v112\n\t"
        "v_exp_f32 v113, v113\n\t"
        "v_exp_f32 v98, v98\n\t"
        "v_exp_f32 v99, v99\n\t"
        "v_mfma_f32_32x32x16_bf16 v[122:137], v[158:161], v[46:49], v[122:137]\n\t"
        "s_waitcnt vmcnt(4)\n\t"
        "v_exp_f32 v100, v100\n\t"
        "v_exp_f32 v101, v101\n\t"
        "v_exp_f32 v102, v102\n\t"
        "v_exp_f32 v103, v103\n\t"
        "v_mfma_f32_32x32x16_bf16 v[138:153], v[158:161], v[54:57], v[138:153]\n\t"
        "s_waitcnt lgkmcnt(0)\n\t"
        "s_barrier\n\t"
        "ds_read_b128 v[154:157], v15 offset:32768\n\t"
        "ds_read_b128 v[158:161], v15 offset:33792\n\t"
        "v_exp_f32 v104, v104\n\t"
        "v_exp_f32 v105, v105\n\t"
        "v_exp_f32 v114, v114\n\t"
        "v_exp_f32 v115, v115\n\t"
        "v_smfmac_f32_32x32x32_bf16 v[58:73], v[162:165], v[90:97], v26\n\t"
        "v_exp_f32 v116, v116\n\t"
        "v_exp_f32 v117, v117\n\t"
        "v_exp_f32 v118, v118\n\t"
        "v_exp_f32 v119, v119\n\t"
        "v_smfmac_f32_32x32x32_bf16 v[74:89], v[162:165], v[106:113], v26\n\t"
        "ds_read_b64_tr_b16 v[162:163], v36 offset:32768\n\t"
        "ds_read_b64_tr_b16 v[164:165], v37 offset:32768\n\t"
        "v_exp_f32 v120, v120\n\t"
        "v_exp_f32 v121, v121\n\t"
        "v_exp_f32 v122, v122\n\t"
        "v_exp_f32 v123, v123\n\t"
        "v_smfmac_f32_32x32x32_bf16 v[58:73], v[170:173], v[98:105], v26\n\t"
        "s_waitcnt lgkmcnt(2)\n\t"
        "s_setprio 3\n\t"
        "v_exp_f32 v124, v124\n\t"
        "v_exp_f32 v125, v125\n\t"
        "v_exp_f32 v126, v126\n\t"
        "v_exp_f32 v127, v127\n\t"
        "v_smfmac_f32_32x32x32_bf16 v[74:89], v[170:173], v[114:121], v26\n\t"
        "ds_read_b64_tr_b16 v[170:171], v36 offset:33792\n\t"
        "ds_read_b64_tr_b16 v[172:173], v37 offset:33792\n\t"
        "v_exp_f32 v128, v128\n\t"
        "v_exp_f32 v129, v129\n\t"
        "v_exp_f32 v138, v138\n\t"
        "v_exp_f32 v139, v139\n\t"
        "v_mfma_f32_32x32x16_bf16 v[90:105], v[154:157], v[42:45], 0\n\t"
        "s_add_u32 m0, s49, 65536\n\t"
        "s_mov_b64 exec, s[52:53]\n\t"
        "buffer_load_dwordx4 v19, s[28:31], s51 offen lds\n\t"
        "s_mov_b64 exec, -1\n\t"
        "v_exp_f32 v140, v140\n\t"
        "v_exp_f32 v141, v141\n\t"
        "v_exp_f32 v142, v142\n\t"
        "v_exp_f32 v143, v143\n\t"
        "v_mfma_f32_32x32x16_bf16 v[106:121], v[154:157], v[50:53], 0\n\t"
        "ds_read_b128 v[154:157], v15 offset:34816\n\t"
        "v_exp_f32 v144, v144\n\t"
        "v_exp_f32 v145, v145\n\t"
        "v_exp_f32 v130, v130\n\t"
        "v_exp_f32 v131, v131\n\t"
        "v_mfma_f32_32x32x16_bf16 v[90:105], v[158:161], v[46:49], v[90:105]\n\t"
        "v_exp_f32 v132, v132\n\t"
        "v_exp_f32 v133, v133\n\t"
        "v_exp_f32 v134, v134\n\t"
        "v_exp_f32 v135, v135\n\t"
        "v_mfma_f32_32x32x16_bf16 v[106:121], v[158:161], v[54:57], v[106:121]\n\t"
        "ds_read_b128 v[158:161], v15 offset:35840\n\t"
        "v_exp_f32 v136, v136\n\t"
        "v_exp_f32 v137, v137\n\t"
        "v_exp_f32 v146, v146\n\t"
        "v_exp_f32 v147, v147\n\t"
        "v_smfmac_f32_32x32x32_bf16 v[58:73], v[166:169], v[122:129], v26\n\t"
        "s_add_u32 m0, s49, 65664\n\t"
        "s_mov_b64 exec, s[52:53]\n\t"
        "buffer_load_dwordx4 v19, s[28:31], s51 offen offset:896 lds\n\t"
        "s_mov_b64 exec, -1\n\t"
        "v_exp_f32 v148, v148\n\t"
        "v_exp_f32 v149, v149\n\t"
        "v_exp_f32 v150, v150\n\t"
        "v_exp_f32 v151, v151\n\t"
        "v_smfmac_f32_32x32x32_bf16 v[74:89], v[166:169], v[138:145], v26\n\t"
        "ds_read_b64_tr_b16 v[166:167], v36 offset:34816\n\t"
        "ds_read_b64_tr_b16 v[168:169], v37 offset:34816\n\t"
        "v_exp_f32 v152, v152\n\t"
        "v_exp_f32 v153, v153\n\t"
        "v_exp_f32 v90, v90\n\t"
        "v_exp_f32 v91, v91\n\t"
        "v_smfmac_f32_32x32x32_bf16 v[58:73], v[174:177], v[130:137], v26\n\t"
        "s_waitcnt lgkmcnt(2)\n\t"
        "v_exp_f32 v92, v92\n\t"
        "v_exp_f32 v93, v93\n\t"
        "v_exp_f32 v94, v94\n\t"
        "v_exp_f32 v95, v95\n\t"
        "v_smfmac_f32_32x32x32_bf16 v[74:89], v[174:177], v[146:153], v26\n\t"
        "ds_read_b64_tr_b16 v[174:175], v36 offset:35840\n\t"
        "ds_read_b64_tr_b16 v[176:177], v37 offset:35840\n\t"
        "v_exp_f32 v96, v96\n\t"
        "v_exp_f32 v97, v97\n\t"
        "v_exp_f32 v106, v106\n\t"
        "v_exp_f32 v107, v107\n\t"
        "v_mfma_f32_32x32x16_bf16 v[122:137], v[154:157], v[42:45], 0\n\t"
        "v_exp_f32 v108, v108\n\t"
        "v_exp_f32 v109, v109\n\t"
        "v_exp_f32 v110, v110\n\t"
        "v_exp_f32 v111, v111\n\t"
        "v_mfma_f32_32x32x16_bf16 v[138:153], v[154:157], v[50:53], 0\n\t"
        "ds_read_b128 v[154:157], v15 offset:36864\n\t"
        "v_exp_f32 v112, v112\n\t"
        "v_exp_f32 v113, v113\n\t"
        "v_exp_f32 v98, v98\n\t"
        "v_exp_f32 v99, v99\n\t"
        "v_mfma_f32_32x32x16_bf16 v[122:137], v[158:161], v[46:49], v[122:137]\n\t"
        "v_exp_f32 v100, v100\n\t"
        "v_exp_f32 v101, v101\n\t"
        "v_exp_f32 v102, v102\n\t"
        "v_exp_f32 v103, v103\n\t"
        "v_mfma_f32_32x32x16_bf16 v[138:153], v[158:161], v[54:57], v[138:153]\n\t"
        "ds_read_b128 v[158:161], v15 offset:37888\n\t"
        "v_exp_f32 v104, v104\n\t"
        "v_exp_f32 v105, v105\n\t"
        "v_exp_f32 v114, v114\n\t"
        "v_exp_f32 v115, v115\n\t"
        "v_smfmac_f32_32x32x32_bf16 v[58:73], v[162:165], v[90:97], v26\n\t"
        "v_exp_f32 v116, v116\n\t"
        "v_exp_f32 v117, v117\n\t"
        "v_exp_f32 v118, v118\n\t"
        "v_exp_f32 v119, v119\n\t"
        "v_smfmac_f32_32x32x32_bf16 v[74:89], v[162:165], v[106:113], v26\n\t"
        "ds_read_b64_tr_b16 v[162:163], v36 offset:36864\n\t"
        "ds_read_b64_tr_b16 v[164:165], v37 offset:36864\n\t"
        "v_exp_f32 v120, v120\n\t"
        "v_exp_f32 v121, v121\n\t"
        "v_exp_f32 v122, v122\n\t"
        "v_exp_f32 v123, v123\n\t"
        "v_smfmac_f32_32x32x32_bf16 v[58:73], v[170:173], v[98:105], v26\n\t"
        "s_waitcnt lgkmcnt(2)\n\t"
        "s_setprio 2\n\t"
        "v_exp_f32 v124, v124\n\t"
        "v_exp_f32 v125, v125\n\t"
        "v_exp_f32 v126, v126\n\t"
        "v_exp_f32 v127, v127\n\t"
        "v_smfmac_f32_32x32x32_bf16 v[74:89], v[170:173], v[114:121], v26\n\t"
        "ds_read_b64_tr_b16 v[170:171], v36 offset:37888\n\t"
        "ds_read_b64_tr_b16 v[172:173], v37 offset:37888\n\t"
        "v_exp_f32 v128, v128\n\t"
        "v_exp_f32 v129, v129\n\t"
        "v_exp_f32 v138, v138\n\t"
        "v_exp_f32 v139, v139\n\t"
        "v_mfma_f32_32x32x16_bf16 v[90:105], v[154:157], v[42:45], 0\n\t"
        "s_waitcnt vmcnt(2)\n\t"
        "v_exp_f32 v140, v140\n\t"
        "v_exp_f32 v141, v141\n\t"
        "v_exp_f32 v142, v142\n\t"
        "v_exp_f32 v143, v143\n\t"
        "v_mfma_f32_32x32x16_bf16 v[106:121], v[154:157], v[50:53], 0\n\t"
        "ds_read_b128 v[154:157], v15 offset:38912\n\t"
        "v_exp_f32 v144, v144\n\t"
        "v_exp_f32 v145, v145\n\t"
        "v_exp_f32 v130, v130\n\t"
        "v_exp_f32 v131, v131\n\t"
        "v_mfma_f32_32x32x16_bf16 v[90:105], v[158:161], v[46:49], v[90:105]\n\t"
        "ds_read_b64 v[178:179], v23\n\t"
        "v_exp_f32 v132, v132\n\t"
        "v_exp_f32 v133, v133\n\t"
        "v_exp_f32 v134, v134\n\t"
        "v_exp_f32 v135, v135\n\t"
        "v_mfma_f32_32x32x16_bf16 v[106:121], v[158:161], v[54:57], v[106:121]\n\t"
        "ds_read_b128 v[158:161], v15 offset:39936\n\t"
        "v_exp_f32 v136, v136\n\t"
        "v_exp_f32 v137, v137\n\t"
        "v_exp_f32 v146, v146\n\t"
        "v_exp_f32 v147, v147\n\t"
        "v_smfmac_f32_32x32x32_bf16 v[58:73], v[166:169], v[122:129], v26\n\t"
        "ds_read_b64 v[180:181], v23 offset:8\n\t"
        "ds_read_b64 v[182:183], v23 offset:16\n\t"
        "v_exp_f32 v148, v148\n\t"
        "v_exp_f32 v149, v149\n\t"
        "v_exp_f32 v150, v150\n\t"
        "v_exp_f32 v151, v151\n\t"
        "v_smfmac_f32_32x32x32_bf16 v[74:89], v[166:169], v[138:145], v26\n\t"
        "ds_read_b64_tr_b16 v[166:167], v36 offset:38912\n\t"
        "ds_read_b64_tr_b16 v[168:169], v37 offset:38912\n\t"
        "v_exp_f32 v152, v152\n\t"
        "v_exp_f32 v153, v153\n\t"
        "v_exp_f32 v90, v90\n\t"
        "v_exp_f32 v91, v91\n\t"
        "v_smfmac_f32_32x32x32_bf16 v[58:73], v[174:177], v[130:137], v26\n\t"
        "s_waitcnt lgkmcnt(4)\n\t"
        "v_exp_f32 v92, v92\n\t"
        "v_exp_f32 v93, v93\n\t"
        "v_exp_f32 v94, v94\n\t"
        "v_exp_f32 v95, v95\n\t"
        "v_smfmac_f32_32x32x32_bf16 v[74:89], v[174:177], v[146:153], v26\n\t"
        "ds_read_b64_tr_b16 v[174:175], v36 offset:39936\n\t"
        "ds_read_b64_tr_b16 v[176:177], v37 offset:39936\n\t"
        "v_exp_f32 v96, v96\n\t"
        "v_exp_f32 v97, v97\n\t"
        "v_exp_f32 v106, v106\n\t"
        "v_exp_f32 v107, v107\n\t"
        "v_mfma_f32_32x32x16_bf16 v[122:137], v[154:157], v[42:45], 0\n\t"
        "ds_read_b64 v[184:185], v23 offset:24\n\t"
        "v_exp_f32 v108, v108\n\t"
        "v_exp_f32 v109, v109\n\t"
        "v_exp_f32 v110, v110\n\t"
        "v_exp_f32 v111, v111\n\t"
        "v_mfma_f32_32x32x16_bf16 v[138:153], v[154:157], v[50:53], 0\n\t"
        "ds_read_b128 v[154:157], v15 offset:40960\n\t"
        "v_exp_f32 v112, v112\n\t"
        "v_exp_f32 v113, v113\n\t"
        "v_exp_f32 v98, v98\n\t"
        "v_exp_f32 v99, v99\n\t"
        "v_mfma_f32_32x32x16_bf16 v[122:137], v[158:161], v[46:49], v[122:137]\n\t"
        "ds_read_b64 v[186:187], v23 offset:32\n\t"
        "v_exp_f32 v100, v100\n\t"
        "v_exp_f32 v101, v101\n\t"
        "v_exp_f32 v102, v102\n\t"
        "v_exp_f32 v103, v103\n\t"
        "v_mfma_f32_32x32x16_bf16 v[138:153], v[158:161], v[54:57], v[138:153]\n\t"
        "ds_read_b128 v[158:161], v15 offset:41984\n\t"
        "v_exp_f32 v104, v104\n\t"
        "v_exp_f32 v105, v105\n\t"
        "v_exp_f32 v114, v114\n\t"
        "v_exp_f32 v115, v115\n\t"
        "v_smfmac_f32_32x32x32_bf16 v[58:73], v[162:165], v[90:97], v26\n\t"
        "ds_read_b64 v[188:189], v23 offset:40\n\t"
        "ds_read_b64 v[190:191], v23 offset:48\n\t"
        "v_exp_f32 v116, v116\n\t"
        "v_exp_f32 v117, v117\n\t"
        "v_exp_f32 v118, v118\n\t"
        "v_exp_f32 v119, v119\n\t"
        "v_smfmac_f32_32x32x32_bf16 v[74:89], v[162:165], v[106:113], v26\n\t"
        "ds_read_b64_tr_b16 v[162:163], v36 offset:40960\n\t"
        "ds_read_b64_tr_b16 v[164:165], v37 offset:40960\n\t"
        "v_exp_f32 v120, v120\n\t"
        "v_exp_f32 v121, v121\n\t"
        "v_exp_f32 v122, v122\n\t"
        "v_exp_f32 v123, v123\n\t"
        "v_smfmac_f32_32x32x32_bf16 v[58:73], v[170:173], v[98:105], v26\n\t"
        "s_waitcnt lgkmcnt(4)\n\t"
        "s_setprio 1\n\t"
        "v_exp_f32 v124, v124\n\t"
        "v_exp_f32 v125, v125\n\t"
        "v_exp_f32 v126, v126\n\t"
        "v_exp_f32 v127, v127\n\t"
        "v_smfmac_f32_32x32x32_bf16 v[74:89], v[170:173], v[114:121], v26\n\t"
        "ds_read_b64_tr_b16 v[170:171], v36 offset:41984\n\t"
        "ds_read_b64_tr_b16 v[172:173], v37 offset:41984\n\t"
        "v_exp_f32 v128, v128\n\t"
        "v_exp_f32 v129, v129\n\t"
        "v_exp_f32 v138, v138\n\t"
        "v_exp_f32 v139, v139\n\t"
        "v_mfma_f32_32x32x16_bf16 v[90:105], v[154:157], v[42:45], 0\n\t"
        "v_exp_f32 v140, v140\n\t"
        "v_exp_f32 v141, v141\n\t"
        "v_exp_f32 v142, v142\n\t"
        "v_exp_f32 v143, v143\n\t"
        "v_mfma_f32_32x32x16_bf16 v[106:121], v[154:157], v[50:53], 0\n\t"
        "s_waitcnt lgkmcnt(4)\n\t"
        "s_cmp_lg_u32 s38, 0\n\t"
        "s_cbranch_scc1 Lqk_skip_%=\n\t"
        "s_setprio 3\n\t"
        "v_dot2_f32_bf16 v208, v178, v178, 0\n\t"
        "v_dot2_f32_bf16 v209, v179, v179, 0\n\t"
        "v_dot2_f32_bf16 v208, v180, v180, v208\n\t"
        "v_dot2_f32_bf16 v209, v181, v181, v209\n\t"
        "v_dot2_f32_bf16 v208, v182, v182, v208\n\t"
        "v_dot2_f32_bf16 v209, v183, v183, v209\n\t"
        "v_dot2_f32_bf16 v208, v184, v184, v208\n\t"
        "v_dot2_f32_bf16 v209, v185, v185, v209\n\t"
        "v_dot2_f32_bf16 v208, v186, v186, v208\n\t"
        "v_dot2_f32_bf16 v209, v187, v187, v209\n\t"
        "v_dot2_f32_bf16 v208, v188, v188, v208\n\t"
        "v_dot2_f32_bf16 v209, v189, v189, v209\n\t"
        "v_dot2_f32_bf16 v208, v190, v190, v208\n\t"
        "v_dot2_f32_bf16 v209, v191, v191, v209\n\t"
        "s_nop 2\n\t"
        "v_add_f32 v208, v208, v209\n\t"
        "v_sqrt_f32 v208, v208\n\t"
        "s_nop 0\n\t"
        "v_add_f32 v208, 0x358637bd, v208\n\t"
        "v_rcp_f32 v208, v208\n\t"
        "s_nop 0\n\t"
        "v_cvt_pk_bf16_f32 v212, v208, 0\n\t"
        "v_cvt_pk_bf16_f32 v213, 0, v208\n\t"
        "v_dot2_f32_bf16 v214, v178, v212, 0\n\t"
        "v_dot2_f32_bf16 v215, v178, v213, 0\n\t"
        "v_dot2_f32_bf16 v216, v179, v212, 0\n\t"
        "v_dot2_f32_bf16 v217, v179, v213, 0\n\t"
        "v_dot2_f32_bf16 v218, v180, v212, 0\n\t"
        "v_dot2_f32_bf16 v219, v180, v213, 0\n\t"
        "v_cvt_pk_bf16_f32 v192, v214, v215\n\t"
        "v_dot2_f32_bf16 v220, v181, v212, 0\n\t"
        "v_dot2_f32_bf16 v221, v181, v213, 0\n\t"
        "v_cvt_pk_bf16_f32 v193, v216, v217\n\t"
        "v_dot2_f32_bf16 v214, v182, v212, 0\n\t"
        "v_dot2_f32_bf16 v215, v182, v213, 0\n\t"
        "v_cvt_pk_bf16_f32 v194, v218, v219\n\t"
        "v_dot2_f32_bf16 v216, v183, v212, 0\n\t"
        "v_dot2_f32_bf16 v217, v183, v213, 0\n\t"
        "v_cvt_pk_bf16_f32 v195, v220, v221\n\t"
        "v_dot2_f32_bf16 v218, v184, v212, 0\n\t"
        "v_dot2_f32_bf16 v219, v184, v213, 0\n\t"
        "v_cvt_pk_bf16_f32 v196, v214, v215\n\t"
        "v_dot2_f32_bf16 v220, v185, v212, 0\n\t"
        "v_dot2_f32_bf16 v221, v185, v213, 0\n\t"
        "v_cvt_pk_bf16_f32 v197, v216, v217\n\t"
        "v_dot2_f32_bf16 v214, v186, v212, 0\n\t"
        "v_dot2_f32_bf16 v215, v186, v213, 0\n\t"
        "v_cvt_pk_bf16_f32 v198, v218, v219\n\t"
        "v_dot2_f32_bf16 v216, v187, v212, 0\n\t"
        "v_dot2_f32_bf16 v217, v187, v213, 0\n\t"
        "v_cvt_pk_bf16_f32 v200, v220, v221\n\t"
        "v_dot2_f32_bf16 v218, v188, v212, 0\n\t"
        "v_dot2_f32_bf16 v219, v188, v213, 0\n\t"
        "v_cvt_pk_bf16_f32 v201, v214, v215\n\t"
        "v_dot2_f32_bf16 v220, v189, v212, 0\n\t"
        "v_dot2_f32_bf16 v221, v189, v213, 0\n\t"
        "v_cvt_pk_bf16_f32 v202, v216, v217\n\t"
        "v_dot2_f32_bf16 v214, v190, v212, 0\n\t"
        "v_dot2_f32_bf16 v215, v190, v213, 0\n\t"
        "v_cvt_pk_bf16_f32 v203, v218, v219\n\t"
        "v_dot2_f32_bf16 v216, v191, v212, 0\n\t"
        "v_dot2_f32_bf16 v217, v191, v213, 0\n\t"
        "v_cvt_pk_bf16_f32 v204, v220, v221\n\t"
        "v_cvt_pk_bf16_f32 v205, v214, v215\n\t"
        "s_nop 0\n\t"
        "v_cvt_pk_bf16_f32 v206, v216, v217\n\t"
        "s_setprio 1\n\t"
        "Lqk_skip_%=:\n\t"
        "ds_read_b128 v[154:157], v15 offset:43008\n\t"
        "v_exp_f32 v144, v144\n\t"
        "v_exp_f32 v145, v145\n\t"
        "v_exp_f32 v130, v130\n\t"
        "v_exp_f32 v131, v131\n\t"
        "v_mfma_f32_32x32x16_bf16 v[90:105], v[158:161], v[46:49], v[90:105]\n\t"
        "ds_write_b128 v33, v[192:195]\n\t"
        "v_exp_f32 v132, v132\n\t"
        "v_exp_f32 v133, v133\n\t"
        "v_exp_f32 v134, v134\n\t"
        "v_exp_f32 v135, v135\n\t"
        "v_mfma_f32_32x32x16_bf16 v[106:121], v[158:161], v[54:57], v[106:121]\n\t"
        "ds_read_b128 v[158:161], v15 offset:44032\n\t"
        "v_exp_f32 v136, v136\n\t"
        "v_exp_f32 v137, v137\n\t"
        "v_exp_f32 v146, v146\n\t"
        "v_exp_f32 v147, v147\n\t"
        "v_smfmac_f32_32x32x32_bf16 v[58:73], v[166:169], v[122:129], v26\n\t"
        "ds_write_b128 v33, v[196:199] offset:1024\n\t"
        "v_exp_f32 v148, v148\n\t"
        "v_exp_f32 v149, v149\n\t"
        "v_exp_f32 v150, v150\n\t"
        "v_exp_f32 v151, v151\n\t"
        "v_smfmac_f32_32x32x32_bf16 v[74:89], v[166:169], v[138:145], v26\n\t"
        "ds_read_b64_tr_b16 v[166:167], v36 offset:43008\n\t"
        "ds_read_b64_tr_b16 v[168:169], v37 offset:43008\n\t"
        "v_exp_f32 v152, v152\n\t"
        "v_exp_f32 v153, v153\n\t"
        "v_exp_f32 v90, v90\n\t"
        "v_exp_f32 v91, v91\n\t"
        "v_smfmac_f32_32x32x32_bf16 v[58:73], v[174:177], v[130:137], v26\n\t"
        "s_waitcnt lgkmcnt(3)\n\t"
        "v_exp_f32 v92, v92\n\t"
        "v_exp_f32 v93, v93\n\t"
        "v_exp_f32 v94, v94\n\t"
        "v_exp_f32 v95, v95\n\t"
        "v_smfmac_f32_32x32x32_bf16 v[74:89], v[174:177], v[146:153], v26\n\t"
        "ds_read_b64_tr_b16 v[174:175], v36 offset:44032\n\t"
        "ds_read_b64_tr_b16 v[176:177], v37 offset:44032\n\t"
        "v_exp_f32 v96, v96\n\t"
        "v_exp_f32 v97, v97\n\t"
        "v_exp_f32 v106, v106\n\t"
        "v_exp_f32 v107, v107\n\t"
        "v_mfma_f32_32x32x16_bf16 v[122:137], v[154:157], v[42:45], 0\n\t"
        "ds_write_b128 v33, v[200:203] offset:512\n\t"
        "v_exp_f32 v108, v108\n\t"
        "v_exp_f32 v109, v109\n\t"
        "v_exp_f32 v110, v110\n\t"
        "v_exp_f32 v111, v111\n\t"
        "v_mfma_f32_32x32x16_bf16 v[138:153], v[154:157], v[50:53], 0\n\t"
        "ds_read_b128 v[154:157], v15 offset:45056\n\t"
        "v_exp_f32 v112, v112\n\t"
        "v_exp_f32 v113, v113\n\t"
        "v_exp_f32 v98, v98\n\t"
        "v_exp_f32 v99, v99\n\t"
        "v_mfma_f32_32x32x16_bf16 v[122:137], v[158:161], v[46:49], v[122:137]\n\t"
        "ds_write_b128 v33, v[204:207] offset:1536\n\t"
        "v_exp_f32 v100, v100\n\t"
        "v_exp_f32 v101, v101\n\t"
        "v_exp_f32 v102, v102\n\t"
        "v_exp_f32 v103, v103\n\t"
        "v_mfma_f32_32x32x16_bf16 v[138:153], v[158:161], v[54:57], v[138:153]\n\t"
        "ds_read_b128 v[158:161], v15 offset:46080\n\t"
        "v_exp_f32 v104, v104\n\t"
        "v_exp_f32 v105, v105\n\t"
        "v_exp_f32 v114, v114\n\t"
        "v_exp_f32 v115, v115\n\t"
        "v_smfmac_f32_32x32x32_bf16 v[58:73], v[162:165], v[90:97], v26\n\t"
        "v_exp_f32 v116, v116\n\t"
        "v_exp_f32 v117, v117\n\t"
        "v_exp_f32 v118, v118\n\t"
        "v_exp_f32 v119, v119\n\t"
        "v_smfmac_f32_32x32x32_bf16 v[74:89], v[162:165], v[106:113], v26\n\t"
        "ds_read_b64_tr_b16 v[162:163], v36 offset:45056\n\t"
        "ds_read_b64_tr_b16 v[164:165], v37 offset:45056\n\t"
        "v_exp_f32 v120, v120\n\t"
        "v_exp_f32 v121, v121\n\t"
        "v_exp_f32 v122, v122\n\t"
        "v_exp_f32 v123, v123\n\t"
        "v_smfmac_f32_32x32x32_bf16 v[58:73], v[170:173], v[98:105], v26\n\t"
        "s_waitcnt lgkmcnt(2)\n\t"
        "s_setprio 0\n\t"
        "v_exp_f32 v124, v124\n\t"
        "v_exp_f32 v125, v125\n\t"
        "v_exp_f32 v126, v126\n\t"
        "v_exp_f32 v127, v127\n\t"
        "v_smfmac_f32_32x32x32_bf16 v[74:89], v[170:173], v[114:121], v26\n\t"
        "ds_read_b64_tr_b16 v[170:171], v36 offset:46080\n\t"
        "ds_read_b64_tr_b16 v[172:173], v37 offset:46080\n\t"
        "v_exp_f32 v128, v128\n\t"
        "v_exp_f32 v129, v129\n\t"
        "v_exp_f32 v138, v138\n\t"
        "v_exp_f32 v139, v139\n\t"
        "v_mfma_f32_32x32x16_bf16 v[90:105], v[154:157], v[42:45], 0\n\t"
        "v_exp_f32 v140, v140\n\t"
        "v_exp_f32 v141, v141\n\t"
        "v_exp_f32 v142, v142\n\t"
        "v_exp_f32 v143, v143\n\t"
        "v_mfma_f32_32x32x16_bf16 v[106:121], v[154:157], v[50:53], 0\n\t"
        "ds_read_b128 v[154:157], v15 offset:47104\n\t"
        "v_exp_f32 v144, v144\n\t"
        "v_exp_f32 v145, v145\n\t"
        "v_exp_f32 v130, v130\n\t"
        "v_exp_f32 v131, v131\n\t"
        "v_mfma_f32_32x32x16_bf16 v[90:105], v[158:161], v[46:49], v[90:105]\n\t"
        "v_exp_f32 v132, v132\n\t"
        "v_exp_f32 v133, v133\n\t"
        "v_exp_f32 v134, v134\n\t"
        "v_exp_f32 v135, v135\n\t"
        "v_mfma_f32_32x32x16_bf16 v[106:121], v[158:161], v[54:57], v[106:121]\n\t"
        "ds_read_b128 v[158:161], v15 offset:48128\n\t"
        "v_exp_f32 v136, v136\n\t"
        "v_exp_f32 v137, v137\n\t"
        "v_exp_f32 v146, v146\n\t"
        "v_exp_f32 v147, v147\n\t"
        "v_smfmac_f32_32x32x32_bf16 v[58:73], v[166:169], v[122:129], v26\n\t"
        "v_exp_f32 v148, v148\n\t"
        "v_exp_f32 v149, v149\n\t"
        "v_exp_f32 v150, v150\n\t"
        "v_exp_f32 v151, v151\n\t"
        "v_smfmac_f32_32x32x32_bf16 v[74:89], v[166:169], v[138:145], v26\n\t"
        "ds_read_b64_tr_b16 v[166:167], v36 offset:47104\n\t"
        "ds_read_b64_tr_b16 v[168:169], v37 offset:47104\n\t"
        "v_exp_f32 v152, v152\n\t"
        "v_exp_f32 v153, v153\n\t"
        "v_exp_f32 v90, v90\n\t"
        "v_exp_f32 v91, v91\n\t"
        "v_smfmac_f32_32x32x32_bf16 v[58:73], v[174:177], v[130:137], v26\n\t"
        "s_waitcnt lgkmcnt(2)\n\t"
        "v_exp_f32 v92, v92\n\t"
        "v_exp_f32 v93, v93\n\t"
        "v_exp_f32 v94, v94\n\t"
        "v_exp_f32 v95, v95\n\t"
        "v_smfmac_f32_32x32x32_bf16 v[74:89], v[174:177], v[146:153], v26\n\t"
        "ds_read_b64_tr_b16 v[174:175], v36 offset:48128\n\t"
        "ds_read_b64_tr_b16 v[176:177], v37 offset:48128\n\t"
        "v_exp_f32 v96, v96\n\t"
        "v_exp_f32 v97, v97\n\t"
        "v_exp_f32 v106, v106\n\t"
        "v_exp_f32 v107, v107\n\t"
        "v_mfma_f32_32x32x16_bf16 v[122:137], v[154:157], v[42:45], 0\n\t"
        "v_exp_f32 v108, v108\n\t"
        "v_exp_f32 v109, v109\n\t"
        "v_exp_f32 v110, v110\n\t"
        "v_exp_f32 v111, v111\n\t"
        "v_mfma_f32_32x32x16_bf16 v[138:153], v[154:157], v[50:53], 0\n\t"
        "v_exp_f32 v112, v112\n\t"
        "v_exp_f32 v113, v113\n\t"
        "v_exp_f32 v98, v98\n\t"
        "v_exp_f32 v99, v99\n\t"
        "v_mfma_f32_32x32x16_bf16 v[122:137], v[158:161], v[46:49], v[122:137]\n\t"
        "s_waitcnt vmcnt(0)\n\t"
        "v_exp_f32 v100, v100\n\t"
        "v_exp_f32 v101, v101\n\t"
        "v_exp_f32 v102, v102\n\t"
        "v_exp_f32 v103, v103\n\t"
        "v_mfma_f32_32x32x16_bf16 v[138:153], v[158:161], v[54:57], v[138:153]\n\t"
        "s_waitcnt lgkmcnt(0)\n\t"
        "s_barrier\n\t"
        "ds_read_b128 v[154:157], v15\n\t"
        "ds_read_b128 v[158:161], v15 offset:1024\n\t"
        "v_exp_f32 v104, v104\n\t"
        "v_exp_f32 v105, v105\n\t"
        "v_exp_f32 v114, v114\n\t"
        "v_exp_f32 v115, v115\n\t"
        "v_smfmac_f32_32x32x32_bf16 v[58:73], v[162:165], v[90:97], v26\n\t"
        "v_exp_f32 v116, v116\n\t"
        "v_exp_f32 v117, v117\n\t"
        "v_exp_f32 v118, v118\n\t"
        "v_exp_f32 v119, v119\n\t"
        "v_smfmac_f32_32x32x32_bf16 v[74:89], v[162:165], v[106:113], v26\n\t"
        "ds_read_b64_tr_b16 v[162:163], v36\n\t"
        "ds_read_b64_tr_b16 v[164:165], v37\n\t"
        "v_exp_f32 v120, v120\n\t"
        "v_exp_f32 v121, v121\n\t"
        "v_exp_f32 v122, v122\n\t"
        "v_exp_f32 v123, v123\n\t"
        "v_smfmac_f32_32x32x32_bf16 v[58:73], v[170:173], v[98:105], v26\n\t"
        "s_waitcnt lgkmcnt(2)\n\t"
        "s_setprio 3\n\t"
        "v_exp_f32 v124, v124\n\t"
        "v_exp_f32 v125, v125\n\t"
        "v_exp_f32 v126, v126\n\t"
        "v_exp_f32 v127, v127\n\t"
        "v_smfmac_f32_32x32x32_bf16 v[74:89], v[170:173], v[114:121], v26\n\t"
        "ds_read_b64_tr_b16 v[170:171], v36 offset:1024\n\t"
        "ds_read_b64_tr_b16 v[172:173], v37 offset:1024\n\t"
        "v_exp_f32 v128, v128\n\t"
        "v_exp_f32 v129, v129\n\t"
        "v_exp_f32 v138, v138\n\t"
        "v_exp_f32 v139, v139\n\t"
        "v_mfma_f32_32x32x16_bf16 v[90:105], v[154:157], v[42:45], 0\n\t"
        "v_exp_f32 v140, v140\n\t"
        "v_exp_f32 v141, v141\n\t"
        "v_exp_f32 v142, v142\n\t"
        "v_exp_f32 v143, v143\n\t"
        "v_mfma_f32_32x32x16_bf16 v[106:121], v[154:157], v[50:53], 0\n\t"
        "ds_read_b128 v[154:157], v15 offset:2048\n\t"
        "v_exp_f32 v144, v144\n\t"
        "v_exp_f32 v145, v145\n\t"
        "v_exp_f32 v130, v130\n\t"
        "v_exp_f32 v131, v131\n\t"
        "v_mfma_f32_32x32x16_bf16 v[90:105], v[158:161], v[46:49], v[90:105]\n\t"
        "v_exp_f32 v132, v132\n\t"
        "v_exp_f32 v133, v133\n\t"
        "v_exp_f32 v134, v134\n\t"
        "v_exp_f32 v135, v135\n\t"
        "v_mfma_f32_32x32x16_bf16 v[106:121], v[158:161], v[54:57], v[106:121]\n\t"
        "ds_read_b128 v[158:161], v15 offset:3072\n\t"
        "v_exp_f32 v136, v136\n\t"
        "v_exp_f32 v137, v137\n\t"
        "v_exp_f32 v146, v146\n\t"
        "v_exp_f32 v147, v147\n\t"
        "v_smfmac_f32_32x32x32_bf16 v[58:73], v[166:169], v[122:129], v26\n\t"
        "s_add_u32 s56, s16, s39\n\t"
        "s_addc_u32 s57, s17, 0\n\t"
        "v_exp_f32 v148, v148\n\t"
        "v_exp_f32 v149, v149\n\t"
        "v_exp_f32 v150, v150\n\t"
        "v_exp_f32 v151, v151\n\t"
        "v_smfmac_f32_32x32x32_bf16 v[74:89], v[166:169], v[138:145], v26\n\t"
        "ds_read_b64_tr_b16 v[166:167], v36 offset:2048\n\t"
        "ds_read_b64_tr_b16 v[168:169], v37 offset:2048\n\t"
        "v_exp_f32 v152, v152\n\t"
        "v_exp_f32 v153, v153\n\t"
        "v_exp_f32 v90, v90\n\t"
        "v_exp_f32 v91, v91\n\t"
        "v_smfmac_f32_32x32x32_bf16 v[58:73], v[174:177], v[130:137], v26\n\t"
        "s_waitcnt lgkmcnt(2)\n\t"
        "s_setprio 2\n\t"
        "v_exp_f32 v92, v92\n\t"
        "v_exp_f32 v93, v93\n\t"
        "v_exp_f32 v94, v94\n\t"
        "v_exp_f32 v95, v95\n\t"
        "v_smfmac_f32_32x32x32_bf16 v[74:89], v[174:177], v[146:153], v26\n\t"
        "ds_read_b64_tr_b16 v[174:175], v36 offset:3072\n\t"
        "ds_read_b64_tr_b16 v[176:177], v37 offset:3072\n\t"
        "v_exp_f32 v96, v96\n\t"
        "v_exp_f32 v97, v97\n\t"
        "v_exp_f32 v106, v106\n\t"
        "v_exp_f32 v107, v107\n\t"
        "v_mfma_f32_32x32x16_bf16 v[122:137], v[154:157], v[42:45], 0\n\t"
        "v_exp_f32 v108, v108\n\t"
        "v_exp_f32 v109, v109\n\t"
        "v_exp_f32 v110, v110\n\t"
        "v_exp_f32 v111, v111\n\t"
        "v_mfma_f32_32x32x16_bf16 v[138:153], v[154:157], v[50:53], 0\n\t"
        "ds_read_b128 v[154:157], v15 offset:4096\n\t"
        "v_exp_f32 v112, v112\n\t"
        "v_exp_f32 v113, v113\n\t"
        "v_exp_f32 v98, v98\n\t"
        "v_exp_f32 v99, v99\n\t"
        "v_mfma_f32_32x32x16_bf16 v[122:137], v[158:161], v[46:49], v[122:137]\n\t"
        "v_exp_f32 v100, v100\n\t"
        "v_exp_f32 v101, v101\n\t"
        "v_exp_f32 v102, v102\n\t"
        "v_exp_f32 v103, v103\n\t"
        "v_mfma_f32_32x32x16_bf16 v[138:153], v[158:161], v[54:57], v[138:153]\n\t"
        "ds_read_b128 v[158:161], v15 offset:5120\n\t"
        "v_exp_f32 v104, v104\n\t"
        "v_exp_f32 v105, v105\n\t"
        "v_exp_f32 v114, v114\n\t"
        "v_exp_f32 v115, v115\n\t"
        "v_smfmac_f32_32x32x32_bf16 v[58:73], v[162:165], v[90:97], v26\n\t"
        "v_exp_f32 v116, v116\n\t"
        "v_exp_f32 v117, v117\n\t"
        "v_exp_f32 v118, v118\n\t"
        "v_exp_f32 v119, v119\n\t"
        "v_smfmac_f32_32x32x32_bf16 v[74:89], v[162:165], v[106:113], v26\n\t"
        "ds_read_b64_tr_b16 v[162:163], v36 offset:4096\n\t"
        "ds_read_b64_tr_b16 v[164:165], v37 offset:4096\n\t"
        "v_exp_f32 v120, v120\n\t"
        "v_exp_f32 v121, v121\n\t"
        "v_exp_f32 v122, v122\n\t"
        "v_exp_f32 v123, v123\n\t"
        "v_smfmac_f32_32x32x32_bf16 v[58:73], v[170:173], v[98:105], v26\n\t"
        "s_waitcnt lgkmcnt(2)\n\t"
        "s_setprio 1\n\t"
        "v_exp_f32 v124, v124\n\t"
        "v_exp_f32 v125, v125\n\t"
        "v_exp_f32 v126, v126\n\t"
        "v_exp_f32 v127, v127\n\t"
        "v_smfmac_f32_32x32x32_bf16 v[74:89], v[170:173], v[114:121], v26\n\t"
        "ds_read_b64_tr_b16 v[170:171], v36 offset:5120\n\t"
        "ds_read_b64_tr_b16 v[172:173], v37 offset:5120\n\t"
        "v_exp_f32 v128, v128\n\t"
        "v_exp_f32 v129, v129\n\t"
        "v_exp_f32 v138, v138\n\t"
        "v_exp_f32 v139, v139\n\t"
        "v_mfma_f32_32x32x16_bf16 v[90:105], v[154:157], v[42:45], 0\n\t"
        "v_exp_f32 v140, v140\n\t"
        "v_exp_f32 v141, v141\n\t"
        "v_exp_f32 v142, v142\n\t"
        "v_exp_f32 v143, v143\n\t"
        "v_mfma_f32_32x32x16_bf16 v[106:121], v[154:157], v[50:53], 0\n\t"
        "ds_read_b128 v[154:157], v15 offset:6144\n\t"
        "v_exp_f32 v144, v144\n\t"
        "v_exp_f32 v145, v145\n\t"
        "v_exp_f32 v130, v130\n\t"
        "v_exp_f32 v131, v131\n\t"
        "v_mfma_f32_32x32x16_bf16 v[90:105], v[158:161], v[46:49], v[90:105]\n\t"
        "v_exp_f32 v132, v132\n\t"
        "v_exp_f32 v133, v133\n\t"
        "v_exp_f32 v134, v134\n\t"
        "v_exp_f32 v135, v135\n\t"
        "v_mfma_f32_32x32x16_bf16 v[106:121], v[158:161], v[54:57], v[106:121]\n\t"
        "ds_read_b128 v[158:161], v15 offset:7168\n\t"
        "v_exp_f32 v136, v136\n\t"
        "v_exp_f32 v137, v137\n\t"
        "v_exp_f32 v146, v146\n\t"
        "v_exp_f32 v147, v147\n\t"
        "v_smfmac_f32_32x32x32_bf16 v[58:73], v[166:169], v[122:129], v26\n\t"
        "v_exp_f32 v148, v148\n\t"
        "v_exp_f32 v149, v149\n\t"
        "v_exp_f32 v150, v150\n\t"
        "v_exp_f32 v151, v151\n\t"
        "v_smfmac_f32_32x32x32_bf16 v[74:89], v[166:169], v[138:145], v26\n\t"
        "ds_read_b64_tr_b16 v[166:167], v36 offset:6144\n\t"
        "ds_read_b64_tr_b16 v[168:169], v37 offset:6144\n\t"
        "v_exp_f32 v152, v152\n\t"
        "v_exp_f32 v153, v153\n\t"
        "v_exp_f32 v90, v90\n\t"
        "v_exp_f32 v91, v91\n\t"
        "v_smfmac_f32_32x32x32_bf16 v[58:73], v[174:177], v[130:137], v26\n\t"
        "s_waitcnt lgkmcnt(2)\n\t"
        "s_setprio 0\n\t"
        "v_exp_f32 v92, v92\n\t"
        "v_exp_f32 v93, v93\n\t"
        "v_exp_f32 v94, v94\n\t"
        "v_exp_f32 v95, v95\n\t"
        "v_smfmac_f32_32x32x32_bf16 v[74:89], v[174:177], v[146:153], v26\n\t"
        "ds_read_b64_tr_b16 v[174:175], v36 offset:7168\n\t"
        "ds_read_b64_tr_b16 v[176:177], v37 offset:7168\n\t"
        "v_exp_f32 v96, v96\n\t"
        "v_exp_f32 v97, v97\n\t"
        "v_exp_f32 v98, v98\n\t"
        "v_exp_f32 v99, v99\n\t"
        "v_mfma_f32_32x32x16_bf16 v[122:137], v[154:157], v[42:45], 0\n\t"
        "v_exp_f32 v100, v100\n\t"
        "v_exp_f32 v101, v101\n\t"
        "v_exp_f32 v102, v102\n\t"
        "v_exp_f32 v103, v103\n\t"
        "v_mfma_f32_32x32x16_bf16 v[122:137], v[158:161], v[46:49], v[122:137]\n\t"
        "v_exp_f32 v104, v104\n\t"
        "v_exp_f32 v105, v105\n\t"
        "v_exp_f32 v106, v106\n\t"
        "v_exp_f32 v107, v107\n\t"
        "v_smfmac_f32_32x32x32_bf16 v[58:73], v[162:165], v[90:97], v26\n\t"
        "v_exp_f32 v108, v108\n\t"
        "v_exp_f32 v109, v109\n\t"
        "v_exp_f32 v110, v110\n\t"
        "v_exp_f32 v111, v111\n\t"
        "v_smfmac_f32_32x32x32_bf16 v[58:73], v[170:173], v[98:105], v26\n\t"
        "v_exp_f32 v112, v112\n\t"
        "v_exp_f32 v113, v113\n\t"
        "v_exp_f32 v114, v114\n\t"
        "v_exp_f32 v115, v115\n\t"
        "v_mfma_f32_32x32x16_bf16 v[138:153], v[154:157], v[50:53], 0\n\t"
        "s_waitcnt lgkmcnt(0)\n\t"
        "v_exp_f32 v122, v122\n\t"
        "v_exp_f32 v123, v123\n\t"
        "v_exp_f32 v124, v124\n\t"
        "v_exp_f32 v125, v125\n\t"
        "v_mfma_f32_32x32x16_bf16 v[138:153], v[158:161], v[54:57], v[138:153]\n\t"
        "v_exp_f32 v126, v126\n\t"
        "v_exp_f32 v127, v127\n\t"
        "v_exp_f32 v128, v128\n\t"
        "v_exp_f32 v129, v129\n\t"
        "v_smfmac_f32_32x32x32_bf16 v[74:89], v[162:165], v[106:113], v26\n\t"
        "v_exp_f32 v130, v130\n\t"
        "v_exp_f32 v131, v131\n\t"
        "v_exp_f32 v132, v132\n\t"
        "v_exp_f32 v133, v133\n\t"
        "v_smfmac_f32_32x32x32_bf16 v[58:73], v[166:169], v[122:129], v26\n\t"
        "v_exp_f32 v134, v134\n\t"
        "v_exp_f32 v135, v135\n\t"
        "v_exp_f32 v136, v136\n\t"
        "v_exp_f32 v137, v137\n\t"
        "v_exp_f32 v116, v116\n\t"
        "v_exp_f32 v117, v117\n\t"
        "v_exp_f32 v118, v118\n\t"
        "v_smfmac_f32_32x32x32_bf16 v[58:73], v[174:177], v[130:137], v26\n\t"
        "v_exp_f32 v119, v119\n\t"
        "v_exp_f32 v120, v120\n\t"
        "v_exp_f32 v121, v121\n\t"
        "v_exp_f32 v138, v138\n\t"
        "v_exp_f32 v139, v139\n\t"
        "v_exp_f32 v140, v140\n\t"
        "v_smfmac_f32_32x32x32_bf16 v[74:89], v[170:173], v[114:121], v26\n\t"
        "v_exp_f32 v141, v141\n\t"
        "v_exp_f32 v142, v142\n\t"
        "v_exp_f32 v143, v143\n\t"
        "v_exp_f32 v144, v144\n\t"
        "v_exp_f32 v145, v145\n\t"
        "v_exp_f32 v146, v146\n\t"
        "ds_write_b128 v21, v[58:61] offset:32768\n\t"
        "v_exp_f32 v147, v147\n\t"
        "v_exp_f32 v148, v148\n\t"
        "v_smfmac_f32_32x32x32_bf16 v[74:89], v[166:169], v[138:145], v26\n\t"
        "ds_write_b128 v21, v[62:65] offset:33792\n\t"
        "v_exp_f32 v149, v149\n\t"
        "v_exp_f32 v150, v150\n\t"
        "ds_write_b128 v21, v[66:69] offset:34816\n\t"
        "v_exp_f32 v151, v151\n\t"
        "v_exp_f32 v152, v152\n\t"
        "v_exp_f32 v153, v153\n\t"
        "ds_write_b128 v21, v[70:73] offset:35840\n\t"
        "s_nop 1\n\t"
        "v_smfmac_f32_32x32x32_bf16 v[74:89], v[174:177], v[146:153], v26\n\t"
        "s_waitcnt lgkmcnt(0)\n\t"
        "s_barrier\n\t"
        "ds_read_b128 v[102:105], v22 offset:35840\n\t"
        "ds_read_b128 v[90:93], v22 offset:32768\n\t"
        "ds_read_b128 v[94:97], v22 offset:33792\n\t"
        "ds_read_b128 v[98:101], v22 offset:34816\n\t"
        "s_waitcnt lgkmcnt(3)\n\t"
        "s_nop 4\n\t"
        "v_pk_add_f32 v[86:87], v[86:87], v[102:103]\n\t"
        "v_pk_add_f32 v[88:89], v[88:89], v[104:105]\n\t"
        "v_mov_b32 v4, v86\n\t"
        "s_waitcnt lgkmcnt(2)\n\t"
        "v_pk_add_f32 v[74:75], v[74:75], v[90:91]\n\t"
        "v_pk_add_f32 v[76:77], v[76:77], v[92:93]\n\t"
        "v_permlane32_swap_b32 v86, v4\n\t"
        "s_waitcnt lgkmcnt(1)\n\t"
        "v_rcp_f32 v4, v4\n\t"
        "v_pk_add_f32 v[78:79], v[78:79], v[94:95]\n\t"
        "v_pk_add_f32 v[80:81], v[80:81], v[96:97]\n\t"
        "s_waitcnt lgkmcnt(0)\n\t"
        "v_pk_add_f32 v[82:83], v[82:83], v[98:99]\n\t"
        "v_pk_add_f32 v[84:85], v[84:85], v[100:101]\n\t"
        "v_pk_mul_f32 v[86:87], v[86:87], v[4:5] op_sel_hi:[1,0]\n\t"
        "v_pk_mul_f32 v[88:89], v[88:89], v[4:5] op_sel_hi:[1,0]\n\t"
        "v_cvt_pk_bf16_f32 v64, v86, v87\n\t"
        "v_cvt_pk_bf16_f32 v65, v88, v89\n\t"
        "ds_write_b64 v24, v[64:65] offset:24\n\t"
        "v_pk_mul_f32 v[74:75], v[74:75], v[4:5] op_sel_hi:[1,0]\n\t"
        "v_pk_mul_f32 v[76:77], v[76:77], v[4:5] op_sel_hi:[1,0]\n\t"
        "v_cvt_pk_bf16_f32 v58, v74, v75\n\t"
        "v_cvt_pk_bf16_f32 v59, v76, v77\n\t"
        "ds_write_b64 v24, v[58:59]\n\t"
        "v_pk_mul_f32 v[78:79], v[78:79], v[4:5] op_sel_hi:[1,0]\n\t"
        "v_pk_mul_f32 v[80:81], v[80:81], v[4:5] op_sel_hi:[1,0]\n\t"
        "v_cvt_pk_bf16_f32 v60, v78, v79\n\t"
        "v_cvt_pk_bf16_f32 v61, v80, v81\n\t"
        "ds_write_b64 v24, v[60:61] offset:8\n\t"
        "v_pk_mul_f32 v[82:83], v[82:83], v[4:5] op_sel_hi:[1,0]\n\t"
        "v_pk_mul_f32 v[84:85], v[84:85], v[4:5] op_sel_hi:[1,0]\n\t"
        "v_cvt_pk_bf16_f32 v62, v82, v83\n\t"
        "v_cvt_pk_bf16_f32 v63, v84, v85\n\t"
        "ds_write_b64 v24, v[62:63] offset:16\n\t"
        "ds_read_b128 v[90:93], v25\n\t"
        "ds_read_b128 v[94:97], v25 offset:1024\n\t"
        "s_waitcnt lgkmcnt(1)\n\t"
        "buffer_store_dwordx4 v[90:93], v20, s[56:59], 0 offen sc0 sc1\n\t"
        "s_mov_b32 exec_hi, 0xffff\n\t"
        "s_waitcnt lgkmcnt(0)\n\t"
        "buffer_store_dwordx4 v[94:97], v20, s[56:59], 0 offen offset:1024 sc0 sc1\n\t"
        "s_endpgm\n\t"
        "Lqk_slow_%=:\n\t"
        "s_waitcnt vmcnt(0) lgkmcnt(0)\n\t"
        "s_nop 15\n\t"
        "Lqk_end_%=:\n\t"
        :
        : [ka] "s"(__builtin_amdgcn_kernarg_segment_ptr()), [bid] "s"(blockIdx.x), [tid] "v"(threadIdx.x),
          [lds] "s"((__attribute__((address_space(3))) char*)lds)
        : "v2", "v3", "v4", "v5", "v6", "v7", "v8", "v9", "v10", "v11", "v12", "v13", "v14", "v15", "v16", "v17", "v18", "v19", "v20", "v21", "v22", "v23", "v24", "v25", "v26", "v27", "v28", "v29", "v30", "v31", "v32", "v33", "v34", "v35", "v36", "v37", "v38", "v39", "v40", "v41", "v42", "v43", "v44", "v45", "v46", "v47", "v48", "v49", "v50", "v51", "v52", "v53", "v54", "v55", "v56", "v57", "v58", "v59", "v60", "v61", "v62", "v63", "v64", "v65", "v66", "v67", "v68", "v69", "v70", "v71", "v72", "v73", "v74", "v75", "v76", "v77", "v78", "v79", "v80", "v81", "v82", "v83", "v84", "v85", "v86", "v87", "v88", "v89", "v90", "v91", "v92", "v93", "v94", "v95", "v96", "v97", "v98", "v99", "v100", "v101", "v102", "v103", "v104", "v105", "v106", "v107", "v108", "v109", "v110", "v111", "v112", "v113", "v114", "v115", "v116", "v117", "v118", "v119", "v120", "v121", "v122", "v123", "v124", "v125", "v126", "v127", "v128", "v129", "v130", "v131", "v132", "v133", "v134", "v135", "v136", "v137", "v138", "v139", "v140", "v141", "v142", "v143", "v144", "v145", "v146", "v147", "v148", "v149", "v150", "v151", "v152", "v153", "v154", "v155", "v156", "v157", "v158", "v159", "v160", "v161", "v162", "v163", "v164", "v165", "v166", "v167", "v168", "v169", "v170", "v171", "v172", "v173", "v174", "v175", "v176", "v177", "v178", "v179", "v180", "v181", "v182", "v183", "v184", "v185", "v186", "v187", "v188", "v189", "v190", "v191", "v192", "v193", "v194", "v195", "v196", "v197", "v198", "v199", "v200", "v201", "v202", "v203", "v204", "v205", "v206", "v207", "v208", "v209", "v210", "v211", "v212", "v213", "v214", "v215", "v216", "v217", "v218", "v219", "v220", "v221", "s3", "s4", "s5", "s6", "s7", "s8", "s9", "s10", "s11", "s12", "s13", "s14", "s15", "s16", "s17", "s18", "s19", "s20", "s21", "s22", "s23", "s24", "s25", "s26", "s27", "s28", "s29", "s30", "s31", "s36", "s37", "s38", "s39", "s40", "s41", "s42", "s43", "s44", "s45", "s46", "s47", "s48", "s49", "s50", "s51", "s52", "s53", "s54", "s55", "s56", "s57", "s58", "s59", "vcc", "scc", "memory");
    }
    const int b = blockIdx.x, bh = (b & 7) * 4 + (b >> 6), qc = (b >> 3) & 7;
    const size_t ho = (size_t)bh * SEQ * 14;
    const float t = __uint_as_float((unsigned)tt[bh & (NH - 1)] << 16);
    slow_path(q + ho, k + ho, v + ho * 2, t, out + ho, qc, lds);
}

extern "C" void kernel_launch(void* const* d_in, const int* in_sizes, int n_in, void* d_out, int out_size, void* d_ws,
                              size_t ws_size, hipStream_t stream) {
    qknorm_attn<<<256, 512, 0, stream>>>((const unsigned*)d_in[0], (const unsigned*)d_in[1], (const unsigned short*)d_in[2],
                                         (const unsigned short*)d_in[3], (unsigned*)d_out);
}
```
